```python
import torch

def _softmax(x, axis=-1):
    return torch.softmax(x, dim=axis)
def _gelu(x):
    return torch.nn.functional.gelu(x, approximate="tanh")
def _silu(x):
    return torch.nn.functional.silu(x)
def _sigmoid(x):
    return torch.sigmoid(x)
def _layernorm(x, axis=-1, eps=1e-5):
    m = x.mean(dim=axis, keepdim=True)
    v = x.var(dim=axis, keepdim=True, unbiased=False)
    return (x - m) / torch.sqrt(v + eps)
def _scatter_add(tgt, idx, src, axis=0):
    return tgt.clone().index_add_(axis, idx, src)
def _segment_sum(src, seg, n):
    out = torch.zeros((n,) + src.shape[1:], dtype=src.dtype)
    return out.index_add_(0, seg, src)
def _pad(x, pw):
    flat = tuple(v for pair in reversed(pw) for v in pair)
    return torch.nn.functional.pad(x, flat)

kernel_name = 'gated_mix__mha_block_cached__gqa_block_cached__decode'

def setup_inputs():

    _w = (lambda a: (lambda t: t.to(torch.bfloat16) if t.is_floating_point() else t)(torch.as_tensor(a)))
    def _A_setup():
        B, Sq, D = 8, 1, 1024
        H, Sk, Dh = 32, 4096, 128
        return {
            "x": _w((lambda *s: torch.randn(*s, dtype=torch.bfloat16) * 0.05)(B, Sq, D)),
            "k_cache": _w((lambda *s: torch.randn(*s, dtype=torch.bfloat16) * 6.4)(B, H, Sk, Dh)),
            "v_cache": _w((lambda *s: torch.randn(*s, dtype=torch.bfloat16) * 0.05)(B, H, Sk, Dh)),
            "wq": _w((lambda *s: torch.randn(*s, dtype=torch.bfloat16) * 6.4)(D, H * Dh) * (D ** -0.5)),
            "wo": _w((lambda *s: torch.randn(*s, dtype=torch.bfloat16) * 0.05)(H * Dh, D) * ((H * Dh) ** -0.5)),
        }
    _A_in = _A_setup()

    def _B_setup():
        B, Sq, D = 8, 1, 1024
        Hq, Hkv, Sk, Dh = 32, 4, 4096, 128
        assert Hq % Hkv == 0, (Hq, Hkv)
        return {
            "x": _w((lambda *s: torch.randn(*s, dtype=torch.bfloat16) * 0.05)(B, Sq, D)),
            "k_cache": _w((lambda *s: torch.randn(*s, dtype=torch.bfloat16) * 6.4)(B, Hkv, Sk, Dh)),
            "v_cache": _w((lambda *s: torch.randn(*s, dtype=torch.bfloat16) * 0.05)(B, Hkv, Sk, Dh)),
            "wq": _w((lambda *s: torch.randn(*s, dtype=torch.bfloat16) * 6.4)(D, Hq * Dh) * (D ** -0.5)),
            "wo": _w((lambda *s: torch.randn(*s, dtype=torch.bfloat16) * 0.05)(Hq * Dh, D) * ((Hq * Dh) ** -0.5)),
        }
    _B_in = _B_setup()

    return {
        "A_x": _A_in["x"],
        "A_k_cache": _A_in["k_cache"],
        "A_v_cache": _A_in["v_cache"],
        "A_wq": _A_in["wq"],
        "A_wo": _A_in["wo"],
        "B_x": _B_in["x"],
        "B_k_cache": _B_in["k_cache"],
        "B_v_cache": _B_in["v_cache"],
        "B_wq": _B_in["wq"],
        "B_wo": _B_in["wo"],
        "gate_w": _w((lambda *s: torch.randn(*s, dtype=torch.bfloat16) * 0.05)(1024) * 16.0),
    }

def reference(A_x, A_k_cache, A_v_cache, A_wq, A_wo, B_x, B_k_cache, B_v_cache, B_wq, B_wo, gate_w):
    def _A_ref(x, k_cache, v_cache, wq, wo):
        B, Sq, D = x.shape
        H, Dh = 32, 128
        q = (lambda x, *d: x.permute(*d))((x @ wq).reshape(B, Sq, H, Dh), 0, 2, 1, 3)
        scores = torch.einsum("bhqd,bhkd->bhqk", q, k_cache) * (Dh ** -0.5)
        p = _softmax(scores, axis=-1)
        o = torch.einsum("bhqk,bhkd->bhqd", p, v_cache)
        return (lambda x, *d: x.permute(*d))(o, 0, 2, 1, 3).reshape(B, Sq, H * Dh) @ wo

    def _B_ref(x, k_cache, v_cache, wq, wo):
        B, Sq, D = x.shape
        Hq, Dh = 32, 128
        Hkv = k_cache.shape[1]
        rep = Hq // Hkv
        q = (lambda x, *d: x.permute(*d))((x @ wq).reshape(B, Sq, Hq, Dh), 0, 2, 1, 3)
        k = (lambda x, n, axis: torch.repeat_interleave(x, n, dim=axis))(k_cache, rep, axis=1)
        v = (lambda x, n, axis: torch.repeat_interleave(x, n, dim=axis))(v_cache, rep, axis=1)
        scores = torch.einsum("bhqd,bhkd->bhqk", q, k) * (Dh ** -0.5)
        p = _softmax(scores, axis=-1)
        o = torch.einsum("bhqk,bhkd->bhqd", p, v)
        return (lambda x, *d: x.permute(*d))(o, 0, 2, 1, 3).reshape(B, Sq, Hq * Dh) @ wo

    x = A_x
    g = _sigmoid((x * gate_w).sum(-1, keepdims=True))
    a = _A_ref(x, A_k_cache, A_v_cache, A_wq, A_wo)
    b = _B_ref(x, B_k_cache, B_v_cache, B_wq, B_wo)
    return g * a + (1 - g) * b
```

```cpp
#include <hip/hip_runtime.h>
#include <stdint.h>

#define A_VALU 1
#define WIN 6
#define ROUND_S 0
#define NA1 4
#define NA2 12

typedef unsigned v4u __attribute__((ext_vector_type(4)));
typedef unsigned v2u __attribute__((ext_vector_type(2)));
typedef float f32x4 __attribute__((ext_vector_type(4)));
typedef float f32x2 __attribute__((ext_vector_type(2)));
typedef __bf16 bf16x8 __attribute__((ext_vector_type(8)));
typedef __bf16 bf16x2 __attribute__((ext_vector_type(2)));
typedef __amdgpu_buffer_rsrc_t rsrc_t;

constexpr int AUX_NT = 2, AUX_SC1 = 16;
constexpr int WS_EPOCH = 0;
constexpr int WS_QPART = 4096;
constexpr int WS_QB = WS_QPART + 2 * 32 * 8 * 8 * 128 * 4;
constexpr int WS_BACC = WS_QB + 8 * 32 * 128 * 4;
constexpr int WS_BML = WS_BACC + 8 * 32 * 8 * 128 * 4;
constexpr int WS_O = WS_BML + 8 * 32 * 8 * 4 * 4;
constexpr int WS_G = WS_O + 2 * 32 * 8 * 128 * 4;
constexpr int WS_Y = WS_G + 32 * 8 * 4 * 4;
constexpr int WS_END = WS_Y + 8 * 32 * 32 * 32 * 4;
constexpr int RSTR = 132;
constexpr unsigned SPIN_CAP = 1u << 20;

struct Params {
  const void* x; const void* kA; const void* vA; const void* wqA; const void* woA;
  const void* kB; const void* vB; const void* wqB; const void* woB; const void* gate;
  void* out; void* ws;
};

__device__ __forceinline__ rsrc_t mk_rsrc(const void* p, size_t off, int bytes) {
  return __builtin_amdgcn_make_buffer_rsrc((void*)((const char*)p + off), 0, bytes, 0x00020000);
}
__device__ __forceinline__ v4u ld_nt(rsrc_t r, int voff, int soff) { return __builtin_amdgcn_raw_buffer_load_b128(r, voff, soff, AUX_NT); }
__device__ __forceinline__ v4u ld_plain(rsrc_t r, int voff, int soff) { return __builtin_amdgcn_raw_buffer_load_b128(r, voff, soff, 0); }
__device__ __forceinline__ v4u ld_sc1(rsrc_t r, int voff) { return __builtin_amdgcn_raw_buffer_load_b128(r, voff, 0, AUX_SC1); }
__device__ __forceinline__ void st_sc1(rsrc_t r, int voff, v4u v) { __builtin_amdgcn_raw_buffer_store_b128(v, r, voff, 0, AUX_SC1); }
template <int N> __device__ __forceinline__ void wait_vm() {
  __builtin_amdgcn_s_waitcnt((N & 15) | ((N >> 4) << 14) | (7 << 4) | (15 << 8));
}
__device__ __forceinline__ void wg_barrier() {
  asm volatile("s_waitcnt lgkmcnt(0)\n\ts_barrier" ::: "memory");
}
#define PIN() do { asm volatile("" ::: "memory"); __builtin_amdgcn_sched_barrier(0); } while (0)

__device__ __forceinline__ float u2f(unsigned u) { return __builtin_bit_cast(float, u); }
__device__ __forceinline__ unsigned f2u(float f) { return __builtin_bit_cast(unsigned, f); }
__device__ __forceinline__ unsigned pk_bf16(float lo, float hi) {
  f32x2 v = {lo, hi};
  return __builtin_bit_cast(unsigned, __builtin_convertvector(v, bf16x2));
}
__device__ __forceinline__ float rnd_bf16(float x) { return u2f(pk_bf16(x, x) & 0xffff0000u); }
__device__ __forceinline__ f32x4 mfma(v4u a, v4u b, f32x4 c) {
  return __builtin_amdgcn_mfma_f32_16x16x32_bf16(__builtin_bit_cast(bf16x8, a), __builtin_bit_cast(bf16x8, b), c, 0, 0, 0);
}
__device__ __forceinline__ float dot2(unsigned a, unsigned b, float c) {
  return __builtin_amdgcn_fdot2_f32_bf16(__builtin_bit_cast(bf16x2, a), __builtin_bit_cast(bf16x2, b), c, false);
}
template <int CTRL> __device__ __forceinline__ float dpp(float x) {
  return u2f((unsigned)__builtin_amdgcn_mov_dpp((int)f2u(x), CTRL, 0xf, 0xf, true));
}
__device__ __forceinline__ float row_allsum(float x) {
  x += dpp<0x128>(x); x += dpp<0x124>(x); x += dpp<0x122>(x); x += dpp<0x121>(x); return x;
}
__device__ __forceinline__ float xrow_max(float x) {
  auto s = __builtin_amdgcn_permlane16_swap(f2u(x), f2u(x), false, false);
  x = fmaxf(u2f(s[0]), u2f(s[1]));
  auto t = __builtin_amdgcn_permlane32_swap(f2u(x), f2u(x), false, false);
  return fmaxf(u2f(t[0]), u2f(t[1]));
}
__device__ __forceinline__ float xrow_sum(float x) {
  auto s = __builtin_amdgcn_permlane16_swap(f2u(x), f2u(x), false, false);
  x = u2f(s[0]) + u2f(s[1]);
  auto t = __builtin_amdgcn_permlane32_swap(f2u(x), f2u(x), false, false);
  return u2f(t[0]) + u2f(t[1]);
}
__device__ __forceinline__ v4u tag_raw(v4u v, unsigned T) {
  v4u r;
  r.x = (v.x & 0xffffff00u) | (T & 0xffu);
  r.y = (v.y & 0xffffff00u) | ((T >> 8) & 0xffu);
  r.z = (v.z & 0xffffff00u) | ((T >> 16) & 0xffu);
  r.w = (v.w & 0xffffff00u) | (T >> 24);
  return r;
}
__device__ __forceinline__ v4u tag_f32(f32x4 v, unsigned T) {
  v4u u = {f2u(v.x) + 0x80u, f2u(v.y) + 0x80u, f2u(v.z) + 0x80u, f2u(v.w) + 0x80u};
  return tag_raw(u, T);
}
__device__ __forceinline__ v4u tag_bf16(f32x4 v, unsigned T) {
  const unsigned a = pk_bf16(v.x, v.y), b = pk_bf16(v.z, v.w);
  v4u u = {a << 16, a & 0xffff0000u, b << 16, b & 0xffff0000u};
  return tag_raw(u, T);
}
__device__ __forceinline__ bool tag_ok(v4u v, unsigned T) {
  const unsigned lo = __builtin_amdgcn_perm(v.y, v.x, 0x0c0c0400u), hi = __builtin_amdgcn_perm(v.w, v.z, 0x04000c0cu);
  return (lo | hi) == T;
}
__device__ __forceinline__ f32x4 untag(v4u v) {
  f32x4 r = {u2f(v.x & 0xffffff00u), u2f(v.y & 0xffffff00u), u2f(v.z & 0xffffff00u), u2f(v.w & 0xffffff00u)};
  return r;
}
__device__ __forceinline__ v4u pack_hi(v4u a, v4u b) {
  v4u r = {__builtin_amdgcn_perm(a.y, a.x, 0x07060302u), __builtin_amdgcn_perm(a.w, a.z, 0x07060302u),
           __builtin_amdgcn_perm(b.y, b.x, 0x07060302u), __builtin_amdgcn_perm(b.w, b.z, 0x07060302u)};
  return r;
}
template <int N, class F> __device__ __forceinline__ void poll(rsrc_t ws, F off, v4u (&v)[N], unsigned T) {
  for (unsigned spin = 0;; ++spin) {
    asm volatile("" ::: "memory");
#pragma unroll
    for (int k = 0; k < N; ++k) v[k] = ld_sc1(ws, off(k));
    bool ok = true;
#pragma unroll
    for (int k = 0; k < N; ++k) ok = ok && tag_ok(v[k], T);
    if (__builtin_amdgcn_ballot_w64(!ok) == 0ull || spin > SPIN_CAP) break;
    __builtin_amdgcn_s_sleep(2);
  }
}
__device__ __forceinline__ void tr_pair(const v4u& r0, const v4u& r1, v4u (&a)[8], int p) {
#pragma unroll
  for (int d = 0; d < 4; ++d) {
    a[2 * d][p] = __builtin_amdgcn_perm(r1[d], r0[d], 0x05040100u);
    a[2 * d + 1][p] = __builtin_amdgcn_perm(r1[d], r0[d], 0x07060302u);
  }
}
__device__ __forceinline__ void tile_to_lds(float* row, const f32x4 (&acc)[8]) {
#pragma unroll
  for (int i = 0; i < 4; ++i) {
    *(f32x4*)(row + 8 * i) = f32x4{acc[0][i], acc[1][i], acc[2][i], acc[3][i]};
    *(f32x4*)(row + 8 * i + 4) = f32x4{acc[4][i], acc[5][i], acc[6][i], acc[7][i]};
  }
}

constexpr float SC_LOG2 = 0.08838834764831845f * 1.4426950408889634f;

template <int MODE, bool HK, bool HV, class FK, class FV>
__device__ __forceinline__ void step(v4u (&K)[8], v4u (&V)[8], const v4u (&Q)[4], float& m, float& l, f32x4 (&acc)[8], FK nk, FV nv) {
  float s[8];
  if constexpr (MODE == 0) {
#pragma unroll
    for (int j = 0; j < 8; ++j) {
      float d = dot2(K[j].x, Q[0].x, 0.f);
      d = dot2(K[j].y, Q[0].y, d); d = dot2(K[j].z, Q[0].z, d); d = dot2(K[j].w, Q[0].w, d);
      asm volatile("" : "+v"(d) :: "memory");
      s[j] = d;
      __builtin_amdgcn_sched_barrier(0);
      if constexpr (HK) { wait_vm<WIN - 1>(); K[j] = nk(j); }
      __builtin_amdgcn_sched_barrier(0);
    }
#pragma unroll
    for (int j = 0; j < 8; ++j) s[j] = row_allsum(s[j]);
  } else {
#pragma unroll
    for (int t = 0; t < 2; ++t) {
      f32x4 d = {0.f, 0.f, 0.f, 0.f};
#pragma unroll
      for (int k = 0; k < 4; ++k) {
        d = mfma(K[4 * t + k], Q[k], d);
        asm volatile("" : "+v"(d) :: "memory");
        __builtin_amdgcn_sched_barrier(0);
        if constexpr (HK) { wait_vm<WIN - 1>(); K[4 * t + k] = nk(4 * t + k); }
        __builtin_amdgcn_sched_barrier(0);
      }
#pragma unroll
      for (int i = 0; i < 4; ++i) s[4 * t + i] = d[i];
    }
  }
#pragma unroll
  for (int j = 0; j < 8; ++j) {
    if (ROUND_S) s[j] = rnd_bf16(rnd_bf16(s[j]) * 0.08838834764831845f) * 1.4426950408889634f;
    else s[j] *= SC_LOG2;
  }
  float mx = fmaxf(fmaxf(fmaxf(s[0], s[1]), fmaxf(s[2], s[3])), fmaxf(fmaxf(s[4], s[5]), fmaxf(s[6], s[7])));
  mx = fmaxf(m, xrow_max(mx));
  const float alpha = __builtin_amdgcn_exp2f(m - mx);
  m = mx;
  float ps = 0.f;
#pragma unroll
  for (int j = 0; j < 8; ++j) { s[j] = __builtin_amdgcn_exp2f(s[j] - mx); ps += s[j]; }
  l = fmaf(l, alpha, ps);
  v4u P = {pk_bf16(s[0], s[1]), pk_bf16(s[2], s[3]), pk_bf16(s[4], s[5]), pk_bf16(s[6], s[7])};
#pragma unroll
  for (int e = 0; e < 8; ++e) acc[e] *= alpha;
  v4u a[8];
#pragma unroll
  for (int p = 0; p < 4; ++p) {
    tr_pair(V[2 * p], V[2 * p + 1], a, p);
    asm volatile("" : "+v"(a[0][p]), "+v"(a[1][p]), "+v"(a[2][p]), "+v"(a[3][p]), "+v"(a[4][p]), "+v"(a[5][p]), "+v"(a[6][p]), "+v"(a[7][p]) :: "memory");
    __builtin_amdgcn_sched_barrier(0);
    if constexpr (HV) {
      wait_vm<WIN - 1>(); V[2 * p] = nv(2 * p);
      __builtin_amdgcn_sched_barrier(0);
      wait_vm<WIN - 1>(); V[2 * p + 1] = nv(2 * p + 1);
    }
    __builtin_amdgcn_sched_barrier(0);
  }
#pragma unroll
  for (int e = 0; e < 8; ++e) acc[e] = mfma(a[e], P, acc[e]);
}

__global__ void __launch_bounds__(256, 1) fused_decode(Params p) {
  __shared__ __attribute__((aligned(16))) float lds[5 * 4 * 8 * RSTR + 2 * 4 * 8 * 2];
  const int tid = threadIdx.x, lane = tid & 63, w = __builtin_amdgcn_readfirstlane(tid >> 6);
  const int g = lane >> 4, c = lane & 15, c7 = lane & 7;
  const int b = blockIdx.x >> 5, h = blockIdx.x & 31;
  float* const red = lds;
  float* const mls = lds + 5 * 4 * 8 * RSTR;

  const rsrc_t rws = mk_rsrc(p.ws, 0, WS_END);
  const rsrc_t rx = mk_rsrc(p.x, 0, 8 * 1024 * 2);
  const rsrc_t rkA = mk_rsrc(p.kA, (size_t)blockIdx.x << 20, 1 << 20);
  const rsrc_t rvA = mk_rsrc(p.vA, (size_t)blockIdx.x << 20, 1 << 20);
  const int lin = lane * 16 + w * 8192;
  const int frg = (4 * (c & 3) + (c >> 2)) * 256 + g * 16 + w * 8192;
  auto ldK_A = [&](int n, int j) -> v4u {
    if (A_VALU) return ld_nt(rkA, lin + 1024 * j, 32768 * n);
    else return ld_nt(rkA, frg + 4096 * (j >> 2) + 64 * (j & 3), 32768 * n);
  };
  auto ldV_A = [&](int n, int j) -> v4u { return ld_nt(rvA, lin + 1024 * j, 32768 * n); };

  v4u K[8], V[8];
  const unsigned ep = __builtin_amdgcn_raw_buffer_load_b32(rws, WS_EPOCH, 0, AUX_SC1);
  const v4u xf = ld_plain(rx, c7 * 2048 + (128 * b + 32 * w + 8 * g) * 2, 0);
  {
    const rsrc_t rqa = mk_rsrc(p.wqA, (size_t)(128 * b) * 8192 + 256 * h, 127 * 8192 + 256);
    const rsrc_t rqb = mk_rsrc(p.wqB, (size_t)(128 * b) * 8192 + 256 * h, 127 * 8192 + 256);
    const int vo = (32 * w + 8 * g) * 8192 + 16 * c;
#pragma unroll
    for (int j = 0; j < 8; ++j) K[j] = ld_nt(rqa, vo, 8192 * j);
#pragma unroll
    for (int j = 0; j < 8; ++j) V[j] = ld_nt(rqb, vo, 8192 * j);
    PIN();
  }
  const unsigned T = (unsigned)__builtin_amdgcn_readfirstlane(ep) + 0x9E3779B1u;
  const int rbt = tid >> 5, rn4 = 4 * (tid & 31);
  auto proj_publish = [&](v4u (&R)[8], int mat) {
    v4u a[8];
#pragma unroll
    for (int q = 0; q < 4; ++q) tr_pair(R[2 * q], R[2 * q + 1], a, q);
    f32x4 d[8];
#pragma unroll
    for (int e = 0; e < 8; ++e) d[e] = mfma(a[e], xf, f32x4{0.f, 0.f, 0.f, 0.f});
    float* rg = red + mat * (4 * 8 * RSTR);
    if (c < 8) tile_to_lds(rg + (w * 8 + c) * RSTR + 32 * g, d);
    wg_barrier();
    f32x4 sum = *(const f32x4*)(rg + rbt * RSTR + rn4);
#pragma unroll
    for (int ww = 1; ww < 4; ++ww) sum += *(const f32x4*)(rg + (ww * 8 + rbt) * RSTR + rn4);
    st_sc1(rws, WS_QPART + ((((mat * 32 + h) * 8 + rbt) * 8 + b) * 128 + rn4) * 4, tag_f32(sum, T));
  };
  proj_publish(K, 0);
  PIN();
#pragma unroll
  for (int j = 0; j < 8; ++j) { wait_vm<WIN - 1>(); K[j] = ldK_A(0, j); __builtin_amdgcn_sched_barrier(0); }
  PIN();
  proj_publish(V, 1);
  PIN();
#pragma unroll
  for (int j = 0; j < 8; ++j) { wait_vm<WIN - 1>(); V[j] = ldV_A(0, j); __builtin_amdgcn_sched_barrier(0); }
  PIN();
  auto gather_q = [&](int mat, float (&q)[8]) {
    v4u u[4];
    const int base = WS_QPART + (((mat * 32 + h) * 8 + b) * 1024 + g * 128 + 8 * c) * 4;
    poll<4>(rws, [&](int k) { return base + (k >> 1) * 2048 + (k & 1) * 16; }, u, T);
    const f32x4 lo = untag(u[0]) + untag(u[2]), hi = untag(u[1]) + untag(u[3]);
#pragma unroll
    for (int i = 0; i < 4; ++i) { q[i] = xrow_sum(lo[i]); q[4 + i] = xrow_sum(hi[i]); }
  };
  v4u QA[4];
  {
    float q[8];
    gather_q(0, q);
    const v4u qq = {pk_bf16(q[0], q[1]), pk_bf16(q[2], q[3]), pk_bf16(q[4], q[5]), pk_bf16(q[6], q[7])};
    if (A_VALU) { QA[0] = qq; QA[1] = qq; QA[2] = qq; QA[3] = qq; }
    else {
#pragma unroll
      for (int s = 0; s < 4; ++s)
#pragma unroll
        for (int i = 0; i < 4; ++i) QA[s][i] = (unsigned)__builtin_amdgcn_ds_bpermute((4 * s + g) * 4, (int)qq[i]);
    }
  }
  float mA = -INFINITY, lA = 0.f;
  f32x4 accA[8];
#pragma unroll
  for (int e = 0; e < 8; ++e) accA[e] = f32x4{0.f, 0.f, 0.f, 0.f};
  auto runA = [&](int n0, int n1) {
    for (int n = n0; n < n1; ++n)
      step<A_VALU ? 0 : 1, true, true>(K, V, QA, mA, lA, accA, [&](int j) { return ldK_A(n + 1, j); }, [&](int j) { return ldV_A(n + 1, j); });
  };
  runA(0, 1);
  if (w == 1) {
    float q[8];
    gather_q(1, q);
    const f32x4 v = (g & 1) ? f32x4{q[4], q[5], q[6], q[7]} : f32x4{q[0], q[1], q[2], q[3]};
    if (g < 2) st_sc1(rws, WS_QB + ((b * 32 + h) * 128 + 8 * c + 4 * g) * 4, tag_bf16(v, T));
  }
  runA(1, NA1 - 1);
  {
    const rsrc_t rkB = mk_rsrc(p.kB, (size_t)blockIdx.x << 17, 1 << 17);
    const rsrc_t rvB = mk_rsrc(p.vB, (size_t)blockIdx.x << 17, 1 << 17);
    auto ldK_B = [&](int n, int j) -> v4u { return ld_nt(rkB, frg + 4096 * (j >> 2) + 64 * (j & 3), 32768 * n); };
    auto ldV_B = [&](int n, int j) -> v4u { return ld_nt(rvB, lin + 1024 * j, 32768 * n); };
    step<A_VALU ? 0 : 1, true, true>(K, V, QA, mA, lA, accA, [&](int j) { return ldK_B(0, j); }, [&](int j) { return ldV_B(0, j); });
    v4u QB[4];
    {
      v4u u[8];
      const int base = WS_QB + ((b * 32 + (h & 24) + c7) * 128 + 8 * g) * 4;
      poll<8>(rws, [&](int k) { return base + (k >> 1) * 128 + (k & 1) * 16; }, u, T);
#pragma unroll
      for (int s = 0; s < 4; ++s) QB[s] = pack_hi(u[2 * s], u[2 * s + 1]);
    }
    float mB = -INFINITY, lB = 0.f;
    f32x4 accB[8];
#pragma unroll
    for (int e = 0; e < 8; ++e) accB[e] = f32x4{0.f, 0.f, 0.f, 0.f};
    for (int n = 0; n < 3; ++n)
      step<1, true, true>(K, V, QB, mB, lB, accB, [&](int j) { return ldK_B(n + 1, j); }, [&](int j) { return ldV_B(n + 1, j); });
    step<1, true, true>(K, V, QB, mB, lB, accB, [&](int j) { return ldK_A(NA1, j); }, [&](int j) { return ldV_A(NA1, j); });
    lB = xrow_sum(lB);
    float* rg = red + 2 * (4 * 8 * RSTR);
    if (c < 8) {
      tile_to_lds(rg + (w * 8 + c) * RSTR + 32 * g, accB);
      if (g == 0) *(f32x2*)(mls + (w * 8 + c) * 2) = f32x2{mB, lB};
    }
    wg_barrier();
    f32x2 ml[4];
#pragma unroll
    for (int ww = 0; ww < 4; ++ww) ml[ww] = *(const f32x2*)(mls + (ww * 8 + rbt) * 2);
    const float M = fmaxf(fmaxf(ml[0].x, ml[1].x), fmaxf(ml[2].x, ml[3].x));
    f32x4 sum = {0.f, 0.f, 0.f, 0.f};
    float L = 0.f;
#pragma unroll
    for (int ww = 0; ww < 4; ++ww) {
      const float wt = __builtin_amdgcn_exp2f(ml[ww].x - M);
      L = fmaf(wt, ml[ww].y, L);
      sum += wt * *(const f32x4*)(rg + (ww * 8 + rbt) * RSTR + rn4);
    }
    const int rec = (b * 32 + (h & 24) + rbt) * 8 + (h & 7);
    st_sc1(rws, WS_BACC + (rec * 128 + rn4) * 4, tag_f32(sum, T));
    if (rn4 == 0) {
      const float Mh = u2f(f2u(M) & 0xffffff00u);
      const v4u r = {f2u(Mh), f2u(M - Mh), f2u(L) + 0x80u, 0u};
      st_sc1(rws, WS_BML + rec * 16, tag_raw(r, T));
    }
  }
  runA(NA1, NA2);
  if (w == 2) {
    const rsrc_t rg = mk_rsrc(p.gate, 0, 2048);
    const v4u x0 = ld_plain(rx, b * 2048 + lane * 32, 0), x1 = ld_plain(rx, b * 2048 + lane * 32 + 16, 0);
    const v4u g0 = ld_plain(rg, lane * 32, 0), g1 = ld_plain(rg, lane * 32 + 16, 0);
    v4u u[5];
    const int rec = (b * 32 + h) * 8;
    const int hf = lane >> 5;
    poll<5>(rws, [&](int k) { return k < 4 ? WS_BACC + ((rec + 2 * k + hf) * 128 + 4 * (lane & 31)) * 4 : WS_BML + (rec + c7) * 16; }, u, T);
    const f32x4 r = untag(u[4]);
    const float ms = r.x + r.y;
    float M = fmaxf(ms, dpp<0x124>(ms)); M = fmaxf(M, dpp<0x122>(M)); M = fmaxf(M, dpp<0x121>(M));
    const float wt = __builtin_amdgcn_exp2f(ms - M);
    float L = wt * r.z; L += dpp<0x124>(L); L += dpp<0x122>(L); L += dpp<0x121>(L);
    f32x4 sum = {0.f, 0.f, 0.f, 0.f};
#pragma unroll
    for (int k = 0; k < 4; ++k) sum += u2f((unsigned)__builtin_amdgcn_ds_bpermute((2 * k + hf) * 4, (int)f2u(wt))) * untag(u[k]);
#pragma unroll
    for (int i = 0; i < 4; ++i) {
      auto t = __builtin_amdgcn_permlane32_swap(f2u(sum[i]), f2u(sum[i]), false, false);
      sum[i] = u2f(t[0]) + u2f(t[1]);
    }
    const float inv = 1.f / L;
    if (lane < 32) st_sc1(rws, WS_O + (((32 + h) * 8 + b) * 128 + 4 * lane) * 4, tag_bf16(sum * inv, T));
    float gs = 0.f;
#pragma unroll
    for (int i = 0; i < 4; ++i) {
      gs += rnd_bf16(u2f(x0[i] << 16) * u2f(g0[i] << 16)) + rnd_bf16(u2f(x0[i] & 0xffff0000u) * u2f(g0[i] & 0xffff0000u));
      gs += rnd_bf16(u2f(x1[i] << 16) * u2f(g1[i] << 16)) + rnd_bf16(u2f(x1[i] & 0xffff0000u) * u2f(g1[i] & 0xffff0000u));
    }
    gs = rnd_bf16(xrow_sum(row_allsum(gs)));
    const float gt = rnd_bf16(1.f / (1.f + __expf(-gs)));
    if (lane == 0) st_sc1(rws, WS_G + (h * 8 + b) * 16, tag_bf16(f32x4{gt, 1.f - gt, 0.f, 0.f}, T));
  }
  runA(NA2, 31);
  {
    const rsrc_t roa = mk_rsrc(p.woA, (size_t)(128 * h) * 2048 + 256 * b, 127 * 2048 + 256);
    const rsrc_t rob = mk_rsrc(p.woB, (size_t)(128 * h) * 2048 + 256 * b, 127 * 2048 + 256);
    const int vo = (32 * w + 8 * g) * 2048 + 16 * c;
    step<A_VALU ? 0 : 1, true, false>(K, V, QA, mA, lA, accA, [&](int j) { return ld_nt(rob, vo, 2048 * j); }, [&](int j) { return V[j]; });
    PIN();
#pragma unroll
    for (int j = 0; j < 8; ++j) V[j] = ld_nt(roa, vo, 2048 * j);
    PIN();
  }
  {
    lA = xrow_sum(lA);
    float* rg = red + 3 * (4 * 8 * RSTR);
    if (c == 0) {
      tile_to_lds(rg + w * RSTR + 32 * g, accA);
      if (g == 0) *(f32x2*)(mls + 64 + w * 2) = f32x2{mA, lA};
    }
    wg_barrier();
    if (w == 0) {
      f32x2 ml[4];
#pragma unroll
      for (int ww = 0; ww < 4; ++ww) ml[ww] = *(const f32x2*)(mls + 64 + ww * 2);
      const float M = fmaxf(fmaxf(ml[0].x, ml[1].x), fmaxf(ml[2].x, ml[3].x));
      f32x4 sum = {0.f, 0.f, 0.f, 0.f};
      float L = 0.f;
#pragma unroll
      for (int ww = 0; ww < 4; ++ww) {
        const float wt = __builtin_amdgcn_exp2f(ml[ww].x - M);
        L = fmaf(wt, ml[ww].y, L);
        sum += wt * *(const f32x4*)(rg + ww * RSTR + rn4);
      }
      if (lane < 32) st_sc1(rws, WS_O + ((h * 8 + b) * 128 + rn4) * 4, tag_bf16(sum * (1.f / L), T));
    }
  }
  {
    f32x4 y[8];
    float gA;
    {
      v4u u[3];
      const int ob = WS_O + (((32 + h) * 8 + c7) * 128 + 32 * w + 8 * g) * 4;
      poll<3>(rws, [&](int k) { return k < 2 ? ob + 16 * k : WS_G + (h * 8 + c7) * 16; }, u, T);
      const v4u oB = pack_hi(u[0], u[1]);
      gA = u2f(u[2].x & 0xffff0000u);
      const float gB = u2f(u[2].y & 0xffff0000u);
      v4u a[8];
#pragma unroll
      for (int q = 0; q < 4; ++q) tr_pair(K[2 * q], K[2 * q + 1], a, q);
#pragma unroll
      for (int e = 0; e < 8; ++e) y[e] = gB * mfma(a[e], oB, f32x4{0.f, 0.f, 0.f, 0.f});
    }
    v4u a[8];
#pragma unroll
    for (int q = 0; q < 4; ++q) tr_pair(V[2 * q], V[2 * q + 1], a, q);
    {
      v4u u[2];
      const int oa = WS_O + ((h * 8 + c7) * 128 + 32 * w + 8 * g) * 4;
      poll<2>(rws, [&](int k) { return oa + 16 * k; }, u, T);
      const v4u oA = pack_hi(u[0], u[1]);
#pragma unroll
      for (int e = 0; e < 8; ++e) y[e] += gA * mfma(a[e], oA, f32x4{0.f, 0.f, 0.f, 0.f});
    }
    float* rg = red + 4 * (4 * 8 * RSTR);
    if (c < 8) tile_to_lds(rg + (w * 8 + c) * RSTR + 32 * g, y);
    wg_barrier();
    f32x4 sum = *(const f32x4*)(rg + rbt * RSTR + rn4);
#pragma unroll
    for (int ww = 1; ww < 4; ++ww) sum += *(const f32x4*)(rg + (ww * 8 + rbt) * RSTR + rn4);
    st_sc1(rws, WS_Y + (((b * 32 + (tid >> 3)) * 32 + h) * 32 + 4 * (tid & 7)) * 4, tag_f32(sum, T));
  }
  if (w == 0) {
    v4u u[4];
    const int yb = WS_Y + (b * 32 + h) * 4096 + lane * 16;
    poll<4>(rws, [&](int k) { return yb + 1024 * k; }, u, T);
    f32x4 sum = (untag(u[0]) + untag(u[1])) + (untag(u[2]) + untag(u[3]));
#pragma unroll
    for (int i = 0; i < 4; ++i) sum[i] = xrow_sum(sum[i] + dpp<0x128>(sum[i]));
    const rsrc_t ro = mk_rsrc(p.out, 0, 8 * 1024 * 2);
    if (lane < 8) {
      const v2u o = {pk_bf16(sum[0], sum[1]), pk_bf16(sum[2], sum[3])};
      __builtin_amdgcn_raw_buffer_store_b64(o, ro, ((h >> 2) * 1024 + 128 * b + 32 * (h & 3) + 4 * lane) * 2, 0, 0);
    }
    if (blockIdx.x == 0 && lane == 0) __builtin_amdgcn_raw_buffer_store_b32(T, rws, WS_EPOCH, 0, AUX_SC1);
  }
}

extern "C" void kernel_launch(void* const* d_in, const int* in_sizes, int n_in, void* d_out, int out_size,
                              void* d_ws, size_t ws_size, hipStream_t stream) {
  Params p;
  p.x = d_in[0]; p.kA = d_in[1]; p.vA = d_in[2]; p.wqA = d_in[3]; p.woA = d_in[4];
  p.kB = d_in[6]; p.vB = d_in[7]; p.wqB = d_in[8]; p.woB = d_in[9]; p.gate = d_in[10];
  p.out = d_out; p.ws = d_ws;
  hipLaunchKernelGGL(fused_decode, dim3(256), dim3(256), 0, stream, p);
}
```

```cpp
#include <hip/hip_runtime.h>
#include <stdint.h>
#include <stdio.h>

#define A_VALU 1
#define WIN 6
#define ROUND_S 1
#define NA1 4
#define WIN0 4
#define PACE 0
#define PACE0 (5 * PACE)

typedef unsigned v4u __attribute__((ext_vector_type(4)));
typedef unsigned v2u __attribute__((ext_vector_type(2)));
typedef float f32x4 __attribute__((ext_vector_type(4)));
typedef float f32x2 __attribute__((ext_vector_type(2)));
typedef __bf16 bf16x8 __attribute__((ext_vector_type(8)));
typedef __bf16 bf16x2 __attribute__((ext_vector_type(2)));
typedef __amdgpu_buffer_rsrc_t rsrc_t;

constexpr int AUX_NT = 2, AUX_SC1 = 16;
#define ST_AUX 16
#define LD_AUX 16
#define QDELAY 0
#define PSLEEP 2
#define WSOFF 0
constexpr int WS_EPOCH = 0;
constexpr int WS_QPART = 4096;
constexpr int WS_QB = WS_QPART + 2 * 32 * 8 * 8 * 128 * 4;
constexpr int WS_BACC = WS_QB + 8 * 32 * 128 * 4;
constexpr int WS_BML = WS_BACC + 8 * 32 * 8 * 128 * 4;
constexpr int WS_O = WS_BML + 8 * 32 * 8 * 4 * 4;
constexpr int WS_G = WS_O + 2 * 32 * 8 * 128 * 4;
constexpr int WS_Y = WS_G + 32 * 8 * 4 * 4;
constexpr int WS_END = WS_Y + 8 * 32 * 32 * 32 * 4;
constexpr int RSTR = 132;
constexpr unsigned SPIN_CAP = 1u << 20;

#define STAMP(i) do {} while (0)

struct Params {
  const void* x; const void* kA; const void* vA; const void* wqA; const void* woA;
  const void* kB; const void* vB; const void* wqB; const void* woB; const void* gate;
  void* out; void* ws;
};

__device__ __forceinline__ rsrc_t mk_rsrc(const void* p, size_t off, int bytes) {
  return __builtin_amdgcn_make_buffer_rsrc((void*)((const char*)p + off), 0, bytes, 0x00020000);
}
__device__ __forceinline__ v4u ld_nt(rsrc_t r, int voff, int soff) { return __builtin_amdgcn_raw_buffer_load_b128(r, voff, soff, AUX_NT); }
__device__ __forceinline__ v4u ld_plain(rsrc_t r, int voff, int soff) { return __builtin_amdgcn_raw_buffer_load_b128(r, voff, soff, 0); }
__device__ __forceinline__ v4u ld_sc1(rsrc_t r, int voff) { return __builtin_amdgcn_raw_buffer_load_b128(r, voff, 0, LD_AUX); }
__device__ __forceinline__ void st_sc1(rsrc_t r, int voff, v4u v) { __builtin_amdgcn_raw_buffer_store_b128(v, r, voff, 0, ST_AUX); }
template <int N> __device__ __forceinline__ void wait_vm() {
  __builtin_amdgcn_s_waitcnt((N & 15) | ((N >> 4) << 14) | (7 << 4) | (15 << 8));
}
__device__ __forceinline__ void wg_barrier() {
  asm volatile("s_waitcnt lgkmcnt(0)\n\ts_barrier" ::: "memory");
}
#define PIN() do { asm volatile("" ::: "memory"); __builtin_amdgcn_sched_barrier(0); } while (0)

__device__ __forceinline__ float u2f(unsigned u) { return __builtin_bit_cast(float, u); }
__device__ __forceinline__ unsigned f2u(float f) { return __builtin_bit_cast(unsigned, f); }
__device__ __forceinline__ unsigned pk_bf16(float lo, float hi) {
  f32x2 v = {lo, hi};
  return __builtin_bit_cast(unsigned, __builtin_convertvector(v, bf16x2));
}
__device__ __forceinline__ float rnd_bf16(float x) { return u2f(pk_bf16(x, x) & 0xffff0000u); }
__device__ __forceinline__ f32x4 mfma(v4u a, v4u b, f32x4 c) {
  return __builtin_amdgcn_mfma_f32_16x16x32_bf16(__builtin_bit_cast(bf16x8, a), __builtin_bit_cast(bf16x8, b), c, 0, 0, 0);
}
__device__ __forceinline__ float dot2(unsigned a, unsigned b, float c) {
  return __builtin_amdgcn_fdot2_f32_bf16(__builtin_bit_cast(bf16x2, a), __builtin_bit_cast(bf16x2, b), c, false);
}
template <int CTRL> __device__ __forceinline__ float dpp(float x) {
  return u2f((unsigned)__builtin_amdgcn_mov_dpp((int)f2u(x), CTRL, 0xf, 0xf, true));
}
__device__ __forceinline__ float row_allsum(float x) {
  x += dpp<0x128>(x); x += dpp<0x124>(x); x += dpp<0x122>(x); x += dpp<0x121>(x); return x;
}
__device__ __forceinline__ float xrow_max(float x) {
  auto s = __builtin_amdgcn_permlane16_swap(f2u(x), f2u(x), false, false);
  x = fmaxf(u2f(s[0]), u2f(s[1]));
  auto t = __builtin_amdgcn_permlane32_swap(f2u(x), f2u(x), false, false);
  return fmaxf(u2f(t[0]), u2f(t[1]));
}
__device__ __forceinline__ float xrow_sum(float x) {
  auto s = __builtin_amdgcn_permlane16_swap(f2u(x), f2u(x), false, false);
  x = u2f(s[0]) + u2f(s[1]);
  auto t = __builtin_amdgcn_permlane32_swap(f2u(x), f2u(x), false, false);
  return u2f(t[0]) + u2f(t[1]);
}
__device__ __forceinline__ v4u tag_raw(v4u v, unsigned T) {
  v4u r;
  r.x = (v.x & 0xffffff00u) | (T & 0xffu);
  r.y = (v.y & 0xffffff00u) | ((T >> 8) & 0xffu);
  r.z = (v.z & 0xffffff00u) | ((T >> 16) & 0xffu);
  r.w = (v.w & 0xffffff00u) | (T >> 24);
  return r;
}
__device__ __forceinline__ v4u tag_f32(f32x4 v, unsigned T) {
  v4u u = {f2u(v.x) + 0x80u, f2u(v.y) + 0x80u, f2u(v.z) + 0x80u, f2u(v.w) + 0x80u};
  return tag_raw(u, T);
}
__device__ __forceinline__ v4u tag_bf16(f32x4 v, unsigned T) {
  const unsigned a = pk_bf16(v.x, v.y), b = pk_bf16(v.z, v.w);
  v4u u = {a << 16, a & 0xffff0000u, b << 16, b & 0xffff0000u};
  return tag_raw(u, T);
}
__device__ __forceinline__ bool tag_ok(v4u v, unsigned T) {
  const unsigned lo = __builtin_amdgcn_perm(v.y, v.x, 0x0c0c0400u), hi = __builtin_amdgcn_perm(v.w, v.z, 0x04000c0cu);
  return (lo | hi) == T;
}
__device__ __forceinline__ f32x4 untag(v4u v) {
  f32x4 r = {u2f(v.x & 0xffffff00u), u2f(v.y & 0xffffff00u), u2f(v.z & 0xffffff00u), u2f(v.w & 0xffffff00u)};
  return r;
}
__device__ __forceinline__ v4u pack_hi(v4u a, v4u b) {
  v4u r = {__builtin_amdgcn_perm(a.y, a.x, 0x07060302u), __builtin_amdgcn_perm(a.w, a.z, 0x07060302u),
           __builtin_amdgcn_perm(b.y, b.x, 0x07060302u), __builtin_amdgcn_perm(b.w, b.z, 0x07060302u)};
  return r;
}
template <int N, class F> __device__ __forceinline__ void poll(rsrc_t ws, F off, v4u (&v)[N], unsigned T) {
  for (unsigned spin = 0;; ++spin) {
    asm volatile("" ::: "memory");
#pragma unroll
    for (int k = 0; k < N; ++k) v[k] = ld_sc1(ws, off(k));
    bool ok = true;
#pragma unroll
    for (int k = 0; k < N; ++k) ok &= tag_ok(v[k], T);
    if (__builtin_amdgcn_ballot_w64(!ok) == 0ull || spin > SPIN_CAP) break;
    __builtin_amdgcn_s_sleep(PSLEEP);
  }
}
__device__ __forceinline__ void tr_pair(const v4u& r0, const v4u& r1, v4u (&a)[8], int p) {
#pragma unroll
  for (int d = 0; d < 4; ++d) {
    a[2 * d][p] = __builtin_amdgcn_perm(r1[d], r0[d], 0x05040100u);
    a[2 * d + 1][p] = __builtin_amdgcn_perm(r1[d], r0[d], 0x07060302u);
  }
}
__device__ __forceinline__ void tile_to_lds(float* row, const f32x4 (&acc)[8]) {
#pragma unroll
  for (int i = 0; i < 4; ++i) {
    *(f32x4*)(row + 8 * i) = f32x4{acc[0][i], acc[1][i], acc[2][i], acc[3][i]};
    *(f32x4*)(row + 8 * i + 4) = f32x4{acc[4][i], acc[5][i], acc[6][i], acc[7][i]};
  }
}

constexpr float SC_LOG2 = 0.08838834764831845f * 1.4426950408889634f;

struct Pacer {
  unsigned t0, acc;
  __device__ __forceinline__ void tick() {
    if (PACE > 0) {
      acc += PACE;
      while ((unsigned)__builtin_amdgcn_s_memrealtime() - t0 < (acc >> 8)) __builtin_amdgcn_s_sleep(1);
    }
  }
};

template <int MODE, bool HK, bool HV, class FK, class FV>
__device__ __forceinline__ void step(v4u (&K)[8], v4u (&V)[8], const v4u (&Q)[4], float& m, float& l, f32x4 (&acc)[8], Pacer& pc, FK nk, FV nv) {
  float s[8];
  if constexpr (MODE == 0) {
#pragma unroll
    for (int j = 0; j < 8; ++j) {
      float d = dot2(K[j].x, Q[0].x, 0.f);
      d = dot2(K[j].y, Q[0].y, d); d = dot2(K[j].z, Q[0].z, d); d = dot2(K[j].w, Q[0].w, d);
      asm volatile("" : "+v"(d) :: "memory");
      s[j] = d;
      __builtin_amdgcn_sched_barrier(0);
      if constexpr (HK) { if ((j & 3) == 0) pc.tick(); wait_vm<WIN - 1>(); K[j] = nk(j); }
      __builtin_amdgcn_sched_barrier(0);
    }
#pragma unroll
    for (int j = 0; j < 8; ++j) s[j] = row_allsum(s[j]);
  } else {
#pragma unroll
    for (int t = 0; t < 2; ++t) {
      f32x4 d = {0.f, 0.f, 0.f, 0.f};
#pragma unroll
      for (int k = 0; k < 4; ++k) {
        d = mfma(K[4 * t + k], Q[k], d);
        asm volatile("" : "+v"(d) :: "memory");
        __builtin_amdgcn_sched_barrier(0);
        if constexpr (HK) { if (k == 0) pc.tick(); wait_vm<WIN - 1>(); K[4 * t + k] = nk(4 * t + k); }
        __builtin_amdgcn_sched_barrier(0);
      }
#pragma unroll
      for (int i = 0; i < 4; ++i) s[4 * t + i] = d[i];
    }
  }
#pragma unroll
  for (int j = 0; j < 8; ++j) {
    if (ROUND_S) s[j] = rnd_bf16(rnd_bf16(s[j]) * 0.08838834764831845f) * 1.4426950408889634f;
    else s[j] *= SC_LOG2;
  }
  float mx = fmaxf(fmaxf(fmaxf(s[0], s[1]), fmaxf(s[2], s[3])), fmaxf(fmaxf(s[4], s[5]), fmaxf(s[6], s[7])));
  mx = fmaxf(m, xrow_max(mx));
  const float alpha = __builtin_amdgcn_exp2f(m - mx);
  m = mx;
  float ps = 0.f;
#pragma unroll
  for (int j = 0; j < 8; ++j) { s[j] = __builtin_amdgcn_exp2f(s[j] - mx); ps += s[j]; }
  l = fmaf(l, alpha, ps);
  v4u P = {pk_bf16(s[0], s[1]), pk_bf16(s[2], s[3]), pk_bf16(s[4], s[5]), pk_bf16(s[6], s[7])};
#pragma unroll
  for (int e = 0; e < 8; ++e) acc[e] *= alpha;
  v4u a[8];
#pragma unroll
  for (int p = 0; p < 4; ++p) {
    tr_pair(V[2 * p], V[2 * p + 1], a, p);
    asm volatile("" : "+v"(a[0][p]), "+v"(a[1][p]), "+v"(a[2][p]), "+v"(a[3][p]), "+v"(a[4][p]), "+v"(a[5][p]), "+v"(a[6][p]), "+v"(a[7][p]) :: "memory");
    __builtin_amdgcn_sched_barrier(0);
    if constexpr (HV) {
      if ((p & 1) == 0) pc.tick();
      wait_vm<WIN - 1>(); V[2 * p] = nv(2 * p);
      __builtin_amdgcn_sched_barrier(0);
      wait_vm<WIN - 1>(); V[2 * p + 1] = nv(2 * p + 1);
    }
    __builtin_amdgcn_sched_barrier(0);
  }
#pragma unroll
  for (int e = 0; e < 8; ++e) acc[e] = mfma(a[e], P, acc[e]);
}

constexpr int L_FLAG = 0;
constexpr int L_T = 16;
constexpr int L_QA = 32;
constexpr int L_QB = L_QA + 64;
constexpr int L_OB = L_QB + 512;
constexpr int L_G = L_OB + 512;
constexpr int L_ML = L_G + 16;
constexpr int L_RB = L_ML + 64 + 16;
constexpr int L_RA = L_RB + 4 * 8 * RSTR;
constexpr int L_RY = L_RA + 4 * RSTR;
constexpr int L_SV = L_RY + 4 * 8 * RSTR;
constexpr int L_END = L_SV + 2 * 8 * RSTR;
constexpr int L_WORDS = 21 * 1024;
static_assert(L_END <= L_WORDS, "lds");

typedef volatile __attribute__((address_space(3))) unsigned lflag_t;
__device__ __forceinline__ void lds_wait(lflag_t* f) {
  for (unsigned spin = 0; __builtin_amdgcn_readfirstlane(*f) == 0u && spin < (SPIN_CAP << 4); ++spin) __builtin_amdgcn_s_sleep(2);
  asm volatile("" ::: "memory");
}
__device__ __forceinline__ void lds_post(lflag_t* f) {
  asm volatile("s_waitcnt lgkmcnt(0)" ::: "memory");
  *f = 1u;
}

__global__ void __launch_bounds__(384, 2) fused_decode(Params p) {
  __shared__ __attribute__((aligned(16))) float lds[L_WORDS];
  const int tid = threadIdx.x, lane = tid & 63, w = __builtin_amdgcn_readfirstlane(tid >> 6);
  const int g = lane >> 4, c = lane & 15, c7 = lane & 7;
  const int b = blockIdx.x >> 5, h = blockIdx.x & 31;
  unsigned* const ldu = (unsigned*)lds;
  lflag_t* const flag = (lflag_t*)lds + L_FLAG;
  const rsrc_t rws = mk_rsrc(p.ws, (size_t)WSOFF, WS_END);
  STAMP(0);
  if (tid < 16) ldu[L_FLAG + tid] = 0u;
  wg_barrier();

  if (w >= 4) {
    __builtin_amdgcn_s_setprio(3);
    const int mat = w - 4;
    const rsrc_t rx = mk_rsrc(p.x, 0, 8 * 1024 * 2);
    if (mat) __builtin_amdgcn_s_sleep(8);
    const unsigned ep = __builtin_amdgcn_raw_buffer_load_b32(rws, WS_EPOCH, 0, AUX_SC1);
    v4u xf[4], W[32];
#pragma unroll
    for (int kb = 0; kb < 4; ++kb) xf[kb] = ld_plain(rx, c7 * 2048 + (128 * b + 32 * kb + 8 * g) * 2, 0);
    {
      const rsrc_t rq = mk_rsrc(mat ? p.wqB : p.wqA, (size_t)(128 * b) * 8192 + 256 * h, 127 * 8192 + 256);
      const int vo = 8 * g * 8192 + 16 * c;
#pragma unroll
      for (int j = 0; j < 32; ++j) W[j] = ld_nt(rq, vo, 8192 * (32 * (j >> 3) + (j & 7)));
      PIN();
    }
    const unsigned T = (unsigned)__builtin_amdgcn_readfirstlane(ep) + 0x9E3779B1u;
    {
      f32x4 d[8];
#pragma unroll
      for (int e = 0; e < 8; ++e) d[e] = f32x4{0.f, 0.f, 0.f, 0.f};
#pragma unroll
      for (int kb = 0; kb < 4; ++kb) {
        v4u a[8];
#pragma unroll
        for (int q = 0; q < 4; ++q) tr_pair(W[8 * kb + 2 * q], W[8 * kb + 2 * q + 1], a, q);
#pragma unroll
        for (int e = 0; e < 8; ++e) d[e] = mfma(a[e], xf[kb], d[e]);
      }
      STAMP(1);
      float* sv = lds + L_SV + mat * 8 * RSTR;
      if (c < 8) tile_to_lds(sv + c * RSTR + 32 * g, d);
      asm volatile("s_waitcnt lgkmcnt(0)" ::: "memory");
#pragma unroll
      for (int k = 0; k < 4; ++k) {
        const int bt = 2 * k + (lane >> 5), n4 = 4 * (lane & 31);
        st_sc1(rws, WS_QPART + ((((mat * 32 + h) * 8 + bt) * 8 + b) * 128 + n4) * 4, tag_f32(*(const f32x4*)(sv + bt * RSTR + n4), T));
      }
    }
    STAMP(2);
    asm volatile("s_waitcnt vmcnt(0)" ::: "memory");
    if (QDELAY > 0) { const unsigned tq = (unsigned)__builtin_amdgcn_s_memrealtime(); while ((unsigned)__builtin_amdgcn_s_memrealtime() - tq < QDELAY) __builtin_amdgcn_s_sleep(2); }
    float q[8];
    {
      v4u u[4];
      const int base = WS_QPART + (((mat * 32 + h) * 8 + b) * 1024 + g * 128 + 8 * c) * 4;
      poll<4>(rws, [&](int k) { return base + (k >> 1) * 2048 + (k & 1) * 16; }, u, T);
      const f32x4 lo = untag(u[0]) + untag(u[2]), hi = untag(u[1]) + untag(u[3]);
#pragma unroll
      for (int i = 0; i < 4; ++i) { q[i] = xrow_sum(lo[i]); q[4 + i] = xrow_sum(hi[i]); }
    }
    if (mat == 0) {
      const v4u qq = {pk_bf16(q[0], q[1]), pk_bf16(q[2], q[3]), pk_bf16(q[4], q[5]), pk_bf16(q[6], q[7])};
      if (g == 0) *(v4u*)(ldu + L_QA + 4 * c) = qq;
      if (lane == 0) ldu[L_T] = T;
      lds_post(flag + 0);
      STAMP(3);
      const rsrc_t roa = mk_rsrc(p.woA, (size_t)(128 * h) * 2048 + 256 * b, 64);
      const rsrc_t rob = mk_rsrc(p.woB, (size_t)(128 * h) * 2048 + 256 * b, 64);
      const rsrc_t ro = mk_rsrc(p.out, 0, 64);
      unsigned t0 = __builtin_amdgcn_raw_buffer_load_b32(roa, 0, 0, AUX_NT);
      unsigned t1 = __builtin_amdgcn_raw_buffer_load_b32(rob, 0, 0, AUX_NT);
      unsigned t2 = __builtin_amdgcn_raw_buffer_load_b32(ro, 0, 0, AUX_NT);
      asm volatile("" :: "v"(t0), "v"(t1), "v"(t2));
    } else {
      {
        const f32x4 v = (g & 1) ? f32x4{q[4], q[5], q[6], q[7]} : f32x4{q[0], q[1], q[2], q[3]};
        if (g < 2) st_sc1(rws, WS_QB + ((b * 32 + h) * 128 + 8 * c + 4 * g) * 4, tag_bf16(v, T));
      }
      float gt;
      {
        const rsrc_t rg = mk_rsrc(p.gate, 0, 2048);
        const v4u x0 = ld_plain(rx, b * 2048 + lane * 32, 0), x1 = ld_plain(rx, b * 2048 + lane * 32 + 16, 0);
        const v4u g0 = ld_plain(rg, lane * 32, 0), g1 = ld_plain(rg, lane * 32 + 16, 0);
        float gs = 0.f;
#pragma unroll
        for (int i = 0; i < 4; ++i) {
          gs += rnd_bf16(u2f(x0[i] << 16) * u2f(g0[i] << 16)) + rnd_bf16(u2f(x0[i] & 0xffff0000u) * u2f(g0[i] & 0xffff0000u));
          gs += rnd_bf16(u2f(x1[i] << 16) * u2f(g1[i] << 16)) + rnd_bf16(u2f(x1[i] & 0xffff0000u) * u2f(g1[i] & 0xffff0000u));
        }
        gs = rnd_bf16(xrow_sum(row_allsum(gs)));
        gt = rnd_bf16(1.f / (1.f + __expf(-gs)));
      }
      if (lane == 0) st_sc1(rws, WS_G + (h * 8 + b) * 16, tag_bf16(f32x4{gt, 1.f - gt, 0.f, 0.f}, T));
      {
        v4u u[4];
        const int base = WS_QB + (b * 32 + (h & 24)) * 512 + lane * 16;
        poll<4>(rws, [&](int k) { return base + 1024 * k; }, u, T);
#pragma unroll
        for (int k = 0; k < 4; ++k)
          *(v2u*)(ldu + L_QB + 128 * k + 2 * lane) = v2u{__builtin_amdgcn_perm(u[k].y, u[k].x, 0x07060302u), __builtin_amdgcn_perm(u[k].w, u[k].z, 0x07060302u)};
        lds_post(flag + 1);
      }
      STAMP(3);
      lds_wait(flag + 4); lds_wait(flag + 5); lds_wait(flag + 6); lds_wait(flag + 7);
      STAMP(4);
#pragma unroll
      for (int k = 0; k < 4; ++k) {
        const int hd = 2 * k + (lane >> 5), n4 = 4 * (lane & 31);
        f32x2 ml[4];
#pragma unroll
        for (int ww = 0; ww < 4; ++ww) ml[ww] = *(const f32x2*)(lds + L_ML + (ww * 8 + hd) * 2);
        const float M = fmaxf(fmaxf(ml[0].x, ml[1].x), fmaxf(ml[2].x, ml[3].x));
        f32x4 sum = {0.f, 0.f, 0.f, 0.f};
        float L = 0.f;
#pragma unroll
        for (int ww = 0; ww < 4; ++ww) {
          const float wt = __builtin_amdgcn_exp2f(ml[ww].x - M);
          L = fmaf(wt, ml[ww].y, L);
          sum += wt * *(const f32x4*)(lds + L_RB + (ww * 8 + hd) * RSTR + n4);
        }
        const int rec = (b * 32 + (h & 24) + hd) * 8 + (h & 7);
        st_sc1(rws, WS_BACC + (rec * 128 + n4) * 4, tag_f32(sum, T));
        if (n4 == 0) {
          const float Mh = u2f(f2u(M) & 0xffffff00u);
          const v4u r = {f2u(Mh), f2u(M - Mh), f2u(L) + 0x80u, 0u};
          st_sc1(rws, WS_BML + rec * 16, tag_raw(r, T));
        }
      }
      {
        v4u u[5];
        const int rec = (b * 32 + h) * 8;
        const int hf = lane >> 5;
        poll<5>(rws, [&](int k) { return k < 4 ? WS_BACC + ((rec + 2 * k + hf) * 128 + 4 * (lane & 31)) * 4 : WS_BML + (rec + c7) * 16; }, u, T);
        const f32x4 r = untag(u[4]);
        const float ms = r.x + r.y;
        float M = fmaxf(ms, dpp<0x124>(ms)); M = fmaxf(M, dpp<0x122>(M)); M = fmaxf(M, dpp<0x121>(M));
        const float wt = __builtin_amdgcn_exp2f(ms - M);
        float L = wt * r.z; L += dpp<0x124>(L); L += dpp<0x122>(L); L += dpp<0x121>(L);
        f32x4 sum = {0.f, 0.f, 0.f, 0.f};
#pragma unroll
        for (int k = 0; k < 4; ++k) sum += u2f((unsigned)__builtin_amdgcn_ds_bpermute((2 * k + hf) * 4, (int)f2u(wt))) * untag(u[k]);
#pragma unroll
        for (int i = 0; i < 4; ++i) {
          auto t = __builtin_amdgcn_permlane32_swap(f2u(sum[i]), f2u(sum[i]), false, false);
          sum[i] = u2f(t[0]) + u2f(t[1]);
        }
        if (lane < 32) st_sc1(rws, WS_O + (((32 + h) * 8 + b) * 128 + 4 * lane) * 4, tag_bf16(sum * (1.f / L), T));
      }
      STAMP(5);
      {
        v4u u[5];
        const int base = WS_O + (32 + h) * 4096 + lane * 16;
        poll<5>(rws, [&](int k) { return k < 4 ? base + 1024 * k : WS_G + (h * 8 + c7) * 16; }, u, T);
#pragma unroll
        for (int k = 0; k < 4; ++k)
          *(v2u*)(ldu + L_OB + 128 * k + 2 * lane) = v2u{__builtin_amdgcn_perm(u[k].y, u[k].x, 0x07060302u), __builtin_amdgcn_perm(u[k].w, u[k].z, 0x07060302u)};
        if (lane < 8) *(v2u*)(ldu + L_G + 2 * lane) = v2u{u[4].x & 0xffff0000u, u[4].y & 0xffff0000u};
        asm volatile("s_waitcnt lgkmcnt(0)" ::: "memory");
      }
      STAMP(6);
    }
  } else {
    Pacer pc;
    pc.t0 = (unsigned)__builtin_amdgcn_s_memrealtime();
    pc.acc = PACE0;
    const rsrc_t rkA = mk_rsrc(p.kA, (size_t)blockIdx.x << 20, 1 << 20);
    const rsrc_t rvA = mk_rsrc(p.vA, (size_t)blockIdx.x << 20, 1 << 20);
    const int lin = lane * 16 + w * 8192;
    const int frg = (4 * (c & 3) + (c >> 2)) * 256 + g * 16 + w * 8192;
    auto ldK_A = [&](int n, int j) -> v4u {
      if (A_VALU) return ld_nt(rkA, lin + 1024 * j, 32768 * n);
      else return ld_nt(rkA, frg + 4096 * (j >> 2) + 64 * (j & 3), 32768 * n);
    };
    auto ldV_A = [&](int n, int j) -> v4u { return ld_nt(rvA, lin + 1024 * j, 32768 * n); };
    v4u K[8], V[8];
    __builtin_amdgcn_s_sleep(8);
#pragma unroll
    for (int j = 0; j < 8; ++j) { wait_vm<WIN0 - 1>(); K[j] = ldK_A(0, j); __builtin_amdgcn_sched_barrier(0); }
#pragma unroll
    for (int j = 0; j < 8; ++j) { wait_vm<WIN0 - 1>(); V[j] = ldV_A(0, j); __builtin_amdgcn_sched_barrier(0); }
    PIN();
    STAMP(2);
    lds_wait(flag + 0);
    v4u QA[4];
    if (A_VALU) { QA[0] = *(const v4u*)(ldu + L_QA + 4 * c); QA[1] = QA[0]; QA[2] = QA[0]; QA[3] = QA[0]; }
    else {
#pragma unroll
      for (int s = 0; s < 4; ++s) QA[s] = *(const v4u*)(ldu + L_QA + 4 * (4 * s + g));
    }
    const unsigned T = (unsigned)__builtin_amdgcn_readfirstlane(ldu[L_T]);
    STAMP(3);
    float mA = -INFINITY, lA = 0.f;
    f32x4 accA[8];
#pragma unroll
    for (int e = 0; e < 8; ++e) accA[e] = f32x4{0.f, 0.f, 0.f, 0.f};
    auto runA = [&](int n0, int n1) {
      for (int n = n0; n < n1; ++n)
        step<A_VALU ? 0 : 1, true, true>(K, V, QA, mA, lA, accA, pc, [&](int j) { return ldK_A(n + 1, j); }, [&](int j) { return ldV_A(n + 1, j); });
    };
    runA(0, NA1 - 1);
    {
      const rsrc_t rkB = mk_rsrc(p.kB, (size_t)blockIdx.x << 17, 1 << 17);
      const rsrc_t rvB = mk_rsrc(p.vB, (size_t)blockIdx.x << 17, 1 << 17);
      auto ldK_B = [&](int n, int j) -> v4u { return ld_nt(rkB, frg + 4096 * (j >> 2) + 64 * (j & 3), 32768 * n); };
      auto ldV_B = [&](int n, int j) -> v4u { return ld_nt(rvB, lin + 1024 * j, 32768 * n); };
      step<A_VALU ? 0 : 1, true, true>(K, V, QA, mA, lA, accA, pc, [&](int j) { return ldK_B(0, j); }, [&](int j) { return ldV_B(0, j); });
      STAMP(4);
      lds_wait(flag + 1);
      v4u QB[4];
#pragma unroll
      for (int s = 0; s < 4; ++s) QB[s] = *(const v4u*)(ldu + L_QB + 64 * c7 + 4 * (4 * s + g));
      STAMP(5);
      float mB = -INFINITY, lB = 0.f;
      f32x4 accB[8];
#pragma unroll
      for (int e = 0; e < 8; ++e) accB[e] = f32x4{0.f, 0.f, 0.f, 0.f};
      for (int n = 0; n < 3; ++n)
        step<1, true, true>(K, V, QB, mB, lB, accB, pc, [&](int j) { return ldK_B(n + 1, j); }, [&](int j) { return ldV_B(n + 1, j); });
      step<1, true, true>(K, V, QB, mB, lB, accB, pc, [&](int j) { return ldK_A(NA1, j); }, [&](int j) { return ldV_A(NA1, j); });
      STAMP(6);
      lB = xrow_sum(lB);
      if (c < 8) {
        tile_to_lds(lds + L_RB + (w * 8 + c) * RSTR + 32 * g, accB);
        if (g == 0) *(f32x2*)(lds + L_ML + (w * 8 + c) * 2) = f32x2{mB, lB};
      }
      lds_post(flag + 4 + w);
    }
    STAMP(7);
    runA(NA1, 31);
    STAMP(10);
    {
      const rsrc_t roa = mk_rsrc(p.woA, (size_t)(128 * h) * 2048 + 256 * b, 127 * 2048 + 256);
      const rsrc_t rob = mk_rsrc(p.woB, (size_t)(128 * h) * 2048 + 256 * b, 127 * 2048 + 256);
      const int vo = (32 * w + 8 * g) * 2048 + 16 * c;
      v4u W[8];
#pragma unroll
      for (int j = 0; j < 8; ++j) W[j] = ld_nt(rob, vo, 2048 * j);
      PIN();
      step<A_VALU ? 0 : 1, false, false>(K, V, QA, mA, lA, accA, pc, [&](int j) { return K[j]; }, [&](int j) { return V[j]; });
      PIN();
#pragma unroll
      for (int j = 0; j < 8; ++j) { V[j] = ld_nt(roa, vo, 2048 * j); K[j] = W[j]; }
      PIN();
    }
    STAMP(11);
    const int rbt = tid >> 5, rn4 = 4 * (tid & 31);
    {
      lA = xrow_sum(lA);
      if (c == 0) {
        tile_to_lds(lds + L_RA + w * RSTR + 32 * g, accA);
        if (g == 0) *(f32x2*)(lds + L_ML + 64 + w * 2) = f32x2{mA, lA};
      }
      wg_barrier();
      if (w == 0) {
        f32x2 ml[4];
#pragma unroll
        for (int ww = 0; ww < 4; ++ww) ml[ww] = *(const f32x2*)(lds + L_ML + 64 + ww * 2);
        const float M = fmaxf(fmaxf(ml[0].x, ml[1].x), fmaxf(ml[2].x, ml[3].x));
        f32x4 sum = {0.f, 0.f, 0.f, 0.f};
        float L = 0.f;
#pragma unroll
        for (int ww = 0; ww < 4; ++ww) {
          const float wt = __builtin_amdgcn_exp2f(ml[ww].x - M);
          L = fmaf(wt, ml[ww].y, L);
          sum += wt * *(const f32x4*)(lds + L_RA + ww * RSTR + rn4);
        }
        if (lane < 32) st_sc1(rws, WS_O + ((h * 8 + b) * 128 + rn4) * 4, tag_bf16(sum * (1.f / L), T));
      }
    }
    STAMP(12);
    {
      f32x4 y[8];
      const f32x2 gt = *(const f32x2*)(lds + L_G + 2 * c7);
      {
        const v4u oB = *(const v4u*)(ldu + L_OB + 64 * c7 + 16 * w + 4 * g);
        v4u a[8];
#pragma unroll
        for (int q = 0; q < 4; ++q) tr_pair(K[2 * q], K[2 * q + 1], a, q);
#pragma unroll
        for (int e = 0; e < 8; ++e) y[e] = gt.y * mfma(a[e], oB, f32x4{0.f, 0.f, 0.f, 0.f});
      }
      v4u a[8];
#pragma unroll
      for (int q = 0; q < 4; ++q) tr_pair(V[2 * q], V[2 * q + 1], a, q);
      {
        v4u u[2];
        const int oa = WS_O + ((h * 8 + c7) * 128 + 32 * w + 8 * g) * 4;
        poll<2>(rws, [&](int k) { return oa + 16 * k; }, u, T);
        const v4u oA = pack_hi(u[0], u[1]);
#pragma unroll
        for (int e = 0; e < 8; ++e) y[e] += gt.x * mfma(a[e], oA, f32x4{0.f, 0.f, 0.f, 0.f});
      }
      STAMP(13);
      if (c < 8) tile_to_lds(lds + L_RY + (w * 8 + c) * RSTR + 32 * g, y);
      wg_barrier();
      f32x4 sum = *(const f32x4*)(lds + L_RY + rbt * RSTR + rn4);
#pragma unroll
      for (int ww = 1; ww < 4; ++ww) sum += *(const f32x4*)(lds + L_RY + (ww * 8 + rbt) * RSTR + rn4);
      st_sc1(rws, WS_Y + (((b * 32 + (tid >> 3)) * 32 + h) * 32 + 4 * (tid & 7)) * 4, tag_f32(sum, T));
    }
    STAMP(14);
    if (w == 0) {
      v4u u[4];
      const int yb = WS_Y + (b * 32 + h) * 4096 + lane * 16;
      poll<4>(rws, [&](int k) { return yb + 1024 * k; }, u, T);
      f32x4 sum = (untag(u[0]) + untag(u[1])) + (untag(u[2]) + untag(u[3]));
#pragma unroll
      for (int i = 0; i < 4; ++i) sum[i] = xrow_sum(sum[i] + dpp<0x128>(sum[i]));
      const rsrc_t ro = mk_rsrc(p.out, 0, 8 * 1024 * 2);
      if (lane < 8) {
        const v2u o = {pk_bf16(sum[0], sum[1]), pk_bf16(sum[2], sum[3])};
        __builtin_amdgcn_raw_buffer_store_b64(o, ro, ((h >> 2) * 1024 + 128 * b + 32 * (h & 3) + 4 * lane) * 2, 0, 0);
      }
      if (blockIdx.x == 0 && lane == 0) __builtin_amdgcn_raw_buffer_store_b32(T, rws, WS_EPOCH, 0, AUX_SC1);
    }
    STAMP(15);
  }
}

extern "C" void kernel_launch(void* const* d_in, const int* in_sizes, int n_in, void* d_out, int out_size,
                              void* d_ws, size_t ws_size, hipStream_t stream) {
  fprintf(stderr, "PTRS x %p kA %p vA %p wqA %p woA %p kB %p vB %p wqB %p woB %p gate %p out %p ws %p\n", d_in[0], d_in[1], d_in[2], d_in[3], d_in[4], d_in[6], d_in[7], d_in[8], d_in[9], d_in[10], d_out, d_ws);
  Params p;
  p.x = d_in[0]; p.kA = d_in[1]; p.vA = d_in[2]; p.wqA = d_in[3]; p.woA = d_in[4];
  p.kB = d_in[6]; p.vB = d_in[7]; p.wqB = d_in[8]; p.woB = d_in[9]; p.gate = d_in[10];
  p.out = d_out; p.ws = d_ws;
  hipLaunchKernelGGL(fused_decode, dim3(256), dim3(384), 0, stream, p);
}
```

```cpp
#include <hip/hip_runtime.h>
#include <stdint.h>

#define A_VALU 1
#define WIN 6
#define ROUND_S 1
#define NA1 4
#define WIN0 4
#define PACE 0
#define PACE0 (5 * PACE)

typedef unsigned v4u __attribute__((ext_vector_type(4)));
typedef unsigned v2u __attribute__((ext_vector_type(2)));
typedef float f32x4 __attribute__((ext_vector_type(4)));
typedef float f32x2 __attribute__((ext_vector_type(2)));
typedef __bf16 bf16x8 __attribute__((ext_vector_type(8)));
typedef __bf16 bf16x2 __attribute__((ext_vector_type(2)));
typedef __amdgpu_buffer_rsrc_t rsrc_t;

constexpr int AUX_NT = 2, AUX_SC1 = 16;
#define ST_AUX 16
#define LD_AUX 16
#define QDELAY 0
#define PSLEEP 2
#define POL_B 2
#define POL_W 2
#define KA_LO 0
#define KA_HI 0
#define VA_LO 0
#define VA_HI 0
#define WSOFF 0
constexpr int WS_EPOCH = 0;
constexpr int WS_QPART = 4096;
constexpr int WS_QB = WS_QPART + 2 * 32 * 8 * 8 * 128 * 4;
constexpr int WS_BACC = WS_QB + 8 * 32 * 128 * 4;
constexpr int WS_BML = WS_BACC + 8 * 32 * 8 * 128 * 4;
constexpr int WS_O = WS_BML + 8 * 32 * 8 * 4 * 4;
constexpr int WS_G = WS_O + 2 * 32 * 8 * 128 * 4;
constexpr int WS_Y = WS_G + 32 * 8 * 4 * 4;
constexpr int WS_END = WS_Y + 8 * 32 * 32 * 32 * 4;
constexpr int RSTR = 132;
constexpr unsigned SPIN_CAP = 1u << 20;

#define STAMP(i) do {} while (0)

struct Params {
  const void* x; const void* kA; const void* vA; const void* wqA; const void* woA;
  const void* kB; const void* vB; const void* wqB; const void* woB; const void* gate;
  void* out; void* ws;
};

__device__ __forceinline__ rsrc_t mk_rsrc(const void* p, size_t off, int bytes) {
  return __builtin_amdgcn_make_buffer_rsrc((void*)((const char*)p + off), 0, bytes, 0x00020000);
}
__device__ __forceinline__ v4u ld_nt(rsrc_t r, int voff, int soff) { return __builtin_amdgcn_raw_buffer_load_b128(r, voff, soff, AUX_NT); }
template <int AUX> __device__ __forceinline__ v4u ld_pol(rsrc_t r, int voff, int soff) { return __builtin_amdgcn_raw_buffer_load_b128(r, voff, soff, AUX); }
__device__ __forceinline__ v4u ld_plain(rsrc_t r, int voff, int soff) { return __builtin_amdgcn_raw_buffer_load_b128(r, voff, soff, 0); }
__device__ __forceinline__ v4u ld_sc1(rsrc_t r, int voff) { return __builtin_amdgcn_raw_buffer_load_b128(r, voff, 0, LD_AUX); }
__device__ __forceinline__ void st_sc1(rsrc_t r, int voff, v4u v) { __builtin_amdgcn_raw_buffer_store_b128(v, r, voff, 0, ST_AUX); }
template <int N> __device__ __forceinline__ void wait_vm() {
  __builtin_amdgcn_s_waitcnt((N & 15) | ((N >> 4) << 14) | (7 << 4) | (15 << 8));
}
__device__ __forceinline__ void wg_barrier() {
  asm volatile("s_waitcnt lgkmcnt(0)\n\ts_barrier" ::: "memory");
}
#define PIN() do { asm volatile("" ::: "memory"); __builtin_amdgcn_sched_barrier(0); } while (0)

__device__ __forceinline__ float u2f(unsigned u) { return __builtin_bit_cast(float, u); }
__device__ __forceinline__ unsigned f2u(float f) { return __builtin_bit_cast(unsigned, f); }
__device__ __forceinline__ unsigned pk_bf16(float lo, float hi) {
  f32x2 v = {lo, hi};
  return __builtin_bit_cast(unsigned, __builtin_convertvector(v, bf16x2));
}
__device__ __forceinline__ float rnd_bf16(float x) { return u2f(pk_bf16(x, x) & 0xffff0000u); }
__device__ __forceinline__ f32x4 mfma(v4u a, v4u b, f32x4 c) {
  return __builtin_amdgcn_mfma_f32_16x16x32_bf16(__builtin_bit_cast(bf16x8, a), __builtin_bit_cast(bf16x8, b), c, 0, 0, 0);
}
__device__ __forceinline__ float dot2(unsigned a, unsigned b, float c) {
  return __builtin_amdgcn_fdot2_f32_bf16(__builtin_bit_cast(bf16x2, a), __builtin_bit_cast(bf16x2, b), c, false);
}
template <int CTRL> __device__ __forceinline__ float dpp(float x) {
  return u2f((unsigned)__builtin_amdgcn_mov_dpp((int)f2u(x), CTRL, 0xf, 0xf, true));
}
__device__ __forceinline__ float row_allsum(float x) {
  x += dpp<0x128>(x); x += dpp<0x124>(x); x += dpp<0x122>(x); x += dpp<0x121>(x); return x;
}
__device__ __forceinline__ float xrow_max(float x) {
  auto s = __builtin_amdgcn_permlane16_swap(f2u(x), f2u(x), false, false);
  x = fmaxf(u2f(s[0]), u2f(s[1]));
  auto t = __builtin_amdgcn_permlane32_swap(f2u(x), f2u(x), false, false);
  return fmaxf(u2f(t[0]), u2f(t[1]));
}
__device__ __forceinline__ float xrow_sum(float x) {
  auto s = __builtin_amdgcn_permlane16_swap(f2u(x), f2u(x), false, false);
  x = u2f(s[0]) + u2f(s[1]);
  auto t = __builtin_amdgcn_permlane32_swap(f2u(x), f2u(x), false, false);
  return u2f(t[0]) + u2f(t[1]);
}
__device__ __forceinline__ v4u tag_raw(v4u v, unsigned T) {
  v4u r;
  r.x = (v.x & 0xffffff00u) | (T & 0xffu);
  r.y = (v.y & 0xffffff00u) | ((T >> 8) & 0xffu);
  r.z = (v.z & 0xffffff00u) | ((T >> 16) & 0xffu);
  r.w = (v.w & 0xffffff00u) | (T >> 24);
  return r;
}
__device__ __forceinline__ v4u tag_f32(f32x4 v, unsigned T) {
  v4u u = {f2u(v.x) + 0x80u, f2u(v.y) + 0x80u, f2u(v.z) + 0x80u, f2u(v.w) + 0x80u};
  return tag_raw(u, T);
}
__device__ __forceinline__ v4u tag_bf16(f32x4 v, unsigned T) {
  const unsigned a = pk_bf16(v.x, v.y), b = pk_bf16(v.z, v.w);
  v4u u = {a << 16, a & 0xffff0000u, b << 16, b & 0xffff0000u};
  return tag_raw(u, T);
}
__device__ __forceinline__ bool tag_ok(v4u v, unsigned T) {
  const unsigned lo = __builtin_amdgcn_perm(v.y, v.x, 0x0c0c0400u), hi = __builtin_amdgcn_perm(v.w, v.z, 0x04000c0cu);
  return (lo | hi) == T;
}
__device__ __forceinline__ f32x4 untag(v4u v) {
  f32x4 r = {u2f(v.x & 0xffffff00u), u2f(v.y & 0xffffff00u), u2f(v.z & 0xffffff00u), u2f(v.w & 0xffffff00u)};
  return r;
}
__device__ __forceinline__ v4u pack_hi(v4u a, v4u b) {
  v4u r = {__builtin_amdgcn_perm(a.y, a.x, 0x07060302u), __builtin_amdgcn_perm(a.w, a.z, 0x07060302u),
           __builtin_amdgcn_perm(b.y, b.x, 0x07060302u), __builtin_amdgcn_perm(b.w, b.z, 0x07060302u)};
  return r;
}
template <int N, class F> __device__ __forceinline__ void poll(rsrc_t ws, F off, v4u (&v)[N], unsigned T) {
  for (unsigned spin = 0;; ++spin) {
    asm volatile("" ::: "memory");
#pragma unroll
    for (int k = 0; k < N; ++k) v[k] = ld_sc1(ws, off(k));
    bool ok = true;
#pragma unroll
    for (int k = 0; k < N; ++k) ok &= tag_ok(v[k], T);
    if (__builtin_amdgcn_ballot_w64(!ok) == 0ull || spin > SPIN_CAP) break;
    __builtin_amdgcn_s_sleep(PSLEEP);
  }
}
__device__ __forceinline__ void tr_pair(const v4u& r0, const v4u& r1, v4u (&a)[8], int p) {
#pragma unroll
  for (int d = 0; d < 4; ++d) {
    a[2 * d][p] = __builtin_amdgcn_perm(r1[d], r0[d], 0x05040100u);
    a[2 * d + 1][p] = __builtin_amdgcn_perm(r1[d], r0[d], 0x07060302u);
  }
}
__device__ __forceinline__ void tile_to_lds(float* row, const f32x4 (&acc)[8]) {
#pragma unroll
  for (int i = 0; i < 4; ++i) {
    *(f32x4*)(row + 8 * i) = f32x4{acc[0][i], acc[1][i], acc[2][i], acc[3][i]};
    *(f32x4*)(row + 8 * i + 4) = f32x4{acc[4][i], acc[5][i], acc[6][i], acc[7][i]};
  }
}

constexpr float SC_LOG2 = 0.08838834764831845f * 1.4426950408889634f;

struct Pacer {
  unsigned t0, acc;
  __device__ __forceinline__ void tick() {
    if (PACE > 0) {
      acc += PACE;
      while ((unsigned)__builtin_amdgcn_s_memrealtime() - t0 < (acc >> 8)) __builtin_amdgcn_s_sleep(1);
    }
  }
};

template <int MODE, bool HK, bool HV, class FK, class FV>
__device__ __forceinline__ void step(v4u (&K)[8], v4u (&V)[8], const v4u (&Q)[4], float& m, float& l, f32x4 (&acc)[8], Pacer& pc, FK nk, FV nv) {
  float s[8];
  if constexpr (MODE == 0) {
#pragma unroll
    for (int j = 0; j < 8; ++j) {
      float d = dot2(K[j].x, Q[0].x, 0.f);
      d = dot2(K[j].y, Q[0].y, d); d = dot2(K[j].z, Q[0].z, d); d = dot2(K[j].w, Q[0].w, d);
      asm volatile("" : "+v"(d) :: "memory");
      s[j] = d;
      __builtin_amdgcn_sched_barrier(0);
      if constexpr (HK) { if ((j & 3) == 0) pc.tick(); wait_vm<WIN - 1>(); K[j] = nk(j); }
      __builtin_amdgcn_sched_barrier(0);
    }
#pragma unroll
    for (int j = 0; j < 8; ++j) s[j] = row_allsum(s[j]);
  } else {
#pragma unroll
    for (int t = 0; t < 2; ++t) {
      f32x4 d = {0.f, 0.f, 0.f, 0.f};
#pragma unroll
      for (int k = 0; k < 4; ++k) {
        d = mfma(K[4 * t + k], Q[k], d);
        asm volatile("" : "+v"(d) :: "memory");
        __builtin_amdgcn_sched_barrier(0);
        if constexpr (HK) { if (k == 0) pc.tick(); wait_vm<WIN - 1>(); K[4 * t + k] = nk(4 * t + k); }
        __builtin_amdgcn_sched_barrier(0);
      }
#pragma unroll
      for (int i = 0; i < 4; ++i) s[4 * t + i] = d[i];
    }
  }
#pragma unroll
  for (int j = 0; j < 8; ++j) {
    if (ROUND_S) s[j] = rnd_bf16(rnd_bf16(s[j]) * 0.08838834764831845f) * 1.4426950408889634f;
    else s[j] *= SC_LOG2;
  }
  float mx = fmaxf(fmaxf(fmaxf(s[0], s[1]), fmaxf(s[2], s[3])), fmaxf(fmaxf(s[4], s[5]), fmaxf(s[6], s[7])));
  mx = fmaxf(m, xrow_max(mx));
  const float alpha = __builtin_amdgcn_exp2f(m - mx);
  m = mx;
  float ps = 0.f;
#pragma unroll
  for (int j = 0; j < 8; ++j) { s[j] = __builtin_amdgcn_exp2f(s[j] - mx); ps += s[j]; }
  l = fmaf(l, alpha, ps);
  v4u P = {pk_bf16(s[0], s[1]), pk_bf16(s[2], s[3]), pk_bf16(s[4], s[5]), pk_bf16(s[6], s[7])};
#pragma unroll
  for (int e = 0; e < 8; ++e) acc[e] *= alpha;
  v4u a[8];
#pragma unroll
  for (int p = 0; p < 4; ++p) {
    tr_pair(V[2 * p], V[2 * p + 1], a, p);
    asm volatile("" : "+v"(a[0][p]), "+v"(a[1][p]), "+v"(a[2][p]), "+v"(a[3][p]), "+v"(a[4][p]), "+v"(a[5][p]), "+v"(a[6][p]), "+v"(a[7][p]) :: "memory");
    __builtin_amdgcn_sched_barrier(0);
    if constexpr (HV) {
      if ((p & 1) == 0) pc.tick();
      wait_vm<WIN - 1>(); V[2 * p] = nv(2 * p);
      __builtin_amdgcn_sched_barrier(0);
      wait_vm<WIN - 1>(); V[2 * p + 1] = nv(2 * p + 1);
    }
    __builtin_amdgcn_sched_barrier(0);
  }
#pragma unroll
  for (int e = 0; e < 8; ++e) acc[e] = mfma(a[e], P, acc[e]);
}

constexpr int L_FLAG = 0;
constexpr int L_T = 16;
constexpr int L_QA = 32;
constexpr int L_QB = L_QA + 64;
constexpr int L_OB = L_QB + 512;
constexpr int L_G = L_OB + 512;
constexpr int L_ML = L_G + 16;
constexpr int L_RB = L_ML + 64 + 16;
constexpr int L_RA = L_RB + 4 * 8 * RSTR;
constexpr int L_RY = L_RA + 4 * RSTR;
constexpr int L_SV = L_RY + 4 * 8 * RSTR;
constexpr int L_END = L_SV + 2 * 8 * RSTR;
constexpr int L_WORDS = 21 * 1024;
static_assert(L_END <= L_WORDS, "lds");

typedef volatile __attribute__((address_space(3))) unsigned lflag_t;
__device__ __forceinline__ void lds_wait(lflag_t* f) {
  for (unsigned spin = 0; __builtin_amdgcn_readfirstlane(*f) == 0u && spin < (SPIN_CAP << 4); ++spin) __builtin_amdgcn_s_sleep(2);
  asm volatile("" ::: "memory");
}
__device__ __forceinline__ void lds_post(lflag_t* f) {
  asm volatile("s_waitcnt lgkmcnt(0)" ::: "memory");
  *f = 1u;
}

__global__ void __launch_bounds__(384, 2) fused_decode(Params p) {
  __shared__ __attribute__((aligned(16))) float lds[L_WORDS];
  const int tid = threadIdx.x, lane = tid & 63, w = __builtin_amdgcn_readfirstlane(tid >> 6);
  const int g = lane >> 4, c = lane & 15, c7 = lane & 7;
  const int b = blockIdx.x >> 5, h = blockIdx.x & 31;
  unsigned* const ldu = (unsigned*)lds;
  lflag_t* const flag = (lflag_t*)lds + L_FLAG;
  const rsrc_t rws = mk_rsrc(p.ws, (size_t)WSOFF, WS_END);
  STAMP(0);
  if (tid < 16) ldu[L_FLAG + tid] = 0u;
  wg_barrier();

  if (w >= 4) {
    __builtin_amdgcn_s_setprio(3);
    const int mat = w - 4;
    const rsrc_t rx = mk_rsrc(p.x, 0, 8 * 1024 * 2);
    if (mat) __builtin_amdgcn_s_sleep(8);
    const unsigned ep = __builtin_amdgcn_raw_buffer_load_b32(rws, WS_EPOCH, 0, AUX_SC1);
    v4u xf[4], W[32];
#pragma unroll
    for (int kb = 0; kb < 4; ++kb) xf[kb] = ld_plain(rx, c7 * 2048 + (128 * b + 32 * kb + 8 * g) * 2, 0);
    {
      const rsrc_t rq = mk_rsrc(mat ? p.wqB : p.wqA, (size_t)(128 * b) * 8192 + 256 * h, 127 * 8192 + 256);
      const int vo = 8 * g * 8192 + 16 * c;
#pragma unroll
      for (int j = 0; j < 32; ++j) W[j] = ld_pol<POL_W>(rq, vo, 8192 * (32 * (j >> 3) + (j & 7)));
      PIN();
    }
    const unsigned T = (unsigned)__builtin_amdgcn_readfirstlane(ep) + 0x9E3779B1u;
    {
      f32x4 d[8];
#pragma unroll
      for (int e = 0; e < 8; ++e) d[e] = f32x4{0.f, 0.f, 0.f, 0.f};
#pragma unroll
      for (int kb = 0; kb < 4; ++kb) {
        v4u a[8];
#pragma unroll
        for (int q = 0; q < 4; ++q) tr_pair(W[8 * kb + 2 * q], W[8 * kb + 2 * q + 1], a, q);
#pragma unroll
        for (int e = 0; e < 8; ++e) d[e] = mfma(a[e], xf[kb], d[e]);
      }
      STAMP(1);
      float* sv = lds + L_SV + mat * 8 * RSTR;
      if (c < 8) tile_to_lds(sv + c * RSTR + 32 * g, d);
      asm volatile("s_waitcnt lgkmcnt(0)" ::: "memory");
#pragma unroll
      for (int k = 0; k < 4; ++k) {
        const int bt = 2 * k + (lane >> 5), n4 = 4 * (lane & 31);
        st_sc1(rws, WS_QPART + ((((mat * 32 + h) * 8 + bt) * 8 + b) * 128 + n4) * 4, tag_f32(*(const f32x4*)(sv + bt * RSTR + n4), T));
      }
    }
    STAMP(2);
    asm volatile("s_waitcnt vmcnt(0)" ::: "memory");
    STAMP(8);
    if (QDELAY > 0) { const unsigned tq = (unsigned)__builtin_amdgcn_s_memrealtime(); while ((unsigned)__builtin_amdgcn_s_memrealtime() - tq < QDELAY) __builtin_amdgcn_s_sleep(2); }
    float q[8];
    {
      v4u u[4];
      const int base = WS_QPART + (((mat * 32 + h) * 8 + b) * 1024 + g * 128 + 8 * c) * 4;
      poll<4>(rws, [&](int k) { return base + (k >> 1) * 2048 + (k & 1) * 16; }, u, T);
      const f32x4 lo = untag(u[0]) + untag(u[2]), hi = untag(u[1]) + untag(u[3]);
#pragma unroll
      for (int i = 0; i < 4; ++i) { q[i] = xrow_sum(lo[i]); q[4 + i] = xrow_sum(hi[i]); }
    }
    if (mat == 0) {
      const v4u qq = {pk_bf16(q[0], q[1]), pk_bf16(q[2], q[3]), pk_bf16(q[4], q[5]), pk_bf16(q[6], q[7])};
      if (g == 0) *(v4u*)(ldu + L_QA + 4 * c) = qq;
      if (lane == 0) ldu[L_T] = T;
      lds_post(flag + 0);
      STAMP(3);
      const rsrc_t roa = mk_rsrc(p.woA, (size_t)(128 * h) * 2048 + 256 * b, 64);
      const rsrc_t rob = mk_rsrc(p.woB, (size_t)(128 * h) * 2048 + 256 * b, 64);
      const rsrc_t ro = mk_rsrc(p.out, 0, 64);
      unsigned t0 = __builtin_amdgcn_raw_buffer_load_b32(roa, 0, 0, AUX_NT);
      unsigned t1 = __builtin_amdgcn_raw_buffer_load_b32(rob, 0, 0, AUX_NT);
      unsigned t2 = __builtin_amdgcn_raw_buffer_load_b32(ro, 0, 0, AUX_NT);
      asm volatile("" :: "v"(t0), "v"(t1), "v"(t2));
    } else {
      {
        const f32x4 v = (g & 1) ? f32x4{q[4], q[5], q[6], q[7]} : f32x4{q[0], q[1], q[2], q[3]};
        if (g < 2) st_sc1(rws, WS_QB + ((b * 32 + h) * 128 + 8 * c + 4 * g) * 4, tag_bf16(v, T));
      }
      float gt;
      {
        const rsrc_t rg = mk_rsrc(p.gate, 0, 2048);
        const v4u x0 = ld_plain(rx, b * 2048 + lane * 32, 0), x1 = ld_plain(rx, b * 2048 + lane * 32 + 16, 0);
        const v4u g0 = ld_plain(rg, lane * 32, 0), g1 = ld_plain(rg, lane * 32 + 16, 0);
        float gs = 0.f;
#pragma unroll
        for (int i = 0; i < 4; ++i) {
          gs += rnd_bf16(u2f(x0[i] << 16) * u2f(g0[i] << 16)) + rnd_bf16(u2f(x0[i] & 0xffff0000u) * u2f(g0[i] & 0xffff0000u));
          gs += rnd_bf16(u2f(x1[i] << 16) * u2f(g1[i] << 16)) + rnd_bf16(u2f(x1[i] & 0xffff0000u) * u2f(g1[i] & 0xffff0000u));
        }
        gs = rnd_bf16(xrow_sum(row_allsum(gs)));
        gt = rnd_bf16(1.f / (1.f + __expf(-gs)));
      }
      if (lane == 0) st_sc1(rws, WS_G + (h * 8 + b) * 16, tag_bf16(f32x4{gt, 1.f - gt, 0.f, 0.f}, T));
      {
        v4u u[4];
        const int base = WS_QB + (b * 32 + (h & 24)) * 512 + lane * 16;
        poll<4>(rws, [&](int k) { return base + 1024 * k; }, u, T);
#pragma unroll
        for (int k = 0; k < 4; ++k)
          *(v2u*)(ldu + L_QB + 128 * k + 2 * lane) = v2u{__builtin_amdgcn_perm(u[k].y, u[k].x, 0x07060302u), __builtin_amdgcn_perm(u[k].w, u[k].z, 0x07060302u)};
        lds_post(flag + 1);
      }
      STAMP(3);
      lds_wait(flag + 4); lds_wait(flag + 5); lds_wait(flag + 6); lds_wait(flag + 7);
      STAMP(4);
#pragma unroll
      for (int k = 0; k < 4; ++k) {
        const int hd = 2 * k + (lane >> 5), n4 = 4 * (lane & 31);
        f32x2 ml[4];
#pragma unroll
        for (int ww = 0; ww < 4; ++ww) ml[ww] = *(const f32x2*)(lds + L_ML + (ww * 8 + hd) * 2);
        const float M = fmaxf(fmaxf(ml[0].x, ml[1].x), fmaxf(ml[2].x, ml[3].x));
        f32x4 sum = {0.f, 0.f, 0.f, 0.f};
        float L = 0.f;
#pragma unroll
        for (int ww = 0; ww < 4; ++ww) {
          const float wt = __builtin_amdgcn_exp2f(ml[ww].x - M);
          L = fmaf(wt, ml[ww].y, L);
          sum += wt * *(const f32x4*)(lds + L_RB + (ww * 8 + hd) * RSTR + n4);
        }
        const int rec = (b * 32 + (h & 24) + hd) * 8 + (h & 7);
        st_sc1(rws, WS_BACC + (rec * 128 + n4) * 4, tag_f32(sum, T));
        if (n4 == 0) {
          const float Mh = u2f(f2u(M) & 0xffffff00u);
          const v4u r = {f2u(Mh), f2u(M - Mh), f2u(L) + 0x80u, 0u};
          st_sc1(rws, WS_BML + rec * 16, tag_raw(r, T));
        }
      }
      {
        v4u u[5];
        const int rec = (b * 32 + h) * 8;
        const int hf = lane >> 5;
        poll<5>(rws, [&](int k) { return k < 4 ? WS_BACC + ((rec + 2 * k + hf) * 128 + 4 * (lane & 31)) * 4 : WS_BML + (rec + c7) * 16; }, u, T);
        const f32x4 r = untag(u[4]);
        const float ms = r.x + r.y;
        float M = fmaxf(ms, dpp<0x124>(ms)); M = fmaxf(M, dpp<0x122>(M)); M = fmaxf(M, dpp<0x121>(M));
        const float wt = __builtin_amdgcn_exp2f(ms - M);
        float L = wt * r.z; L += dpp<0x124>(L); L += dpp<0x122>(L); L += dpp<0x121>(L);
        f32x4 sum = {0.f, 0.f, 0.f, 0.f};
#pragma unroll
        for (int k = 0; k < 4; ++k) sum += u2f((unsigned)__builtin_amdgcn_ds_bpermute((2 * k + hf) * 4, (int)f2u(wt))) * untag(u[k]);
#pragma unroll
        for (int i = 0; i < 4; ++i) {
          auto t = __builtin_amdgcn_permlane32_swap(f2u(sum[i]), f2u(sum[i]), false, false);
          sum[i] = u2f(t[0]) + u2f(t[1]);
        }
        if (lane < 32) st_sc1(rws, WS_O + (((32 + h) * 8 + b) * 128 + 4 * lane) * 4, tag_bf16(sum * (1.f / L), T));
      }
      STAMP(5);
      {
        v4u u[5];
        const int base = WS_O + (32 + h) * 4096 + lane * 16;
        poll<5>(rws, [&](int k) { return k < 4 ? base + 1024 * k : WS_G + (h * 8 + c7) * 16; }, u, T);
#pragma unroll
        for (int k = 0; k < 4; ++k)
          *(v2u*)(ldu + L_OB + 128 * k + 2 * lane) = v2u{__builtin_amdgcn_perm(u[k].y, u[k].x, 0x07060302u), __builtin_amdgcn_perm(u[k].w, u[k].z, 0x07060302u)};
        if (lane < 8) *(v2u*)(ldu + L_G + 2 * lane) = v2u{u[4].x & 0xffff0000u, u[4].y & 0xffff0000u};
        asm volatile("s_waitcnt lgkmcnt(0)" ::: "memory");
      }
      STAMP(6);
    }
  } else {
    Pacer pc;
    pc.t0 = (unsigned)__builtin_amdgcn_s_memrealtime();
    pc.acc = PACE0;
    const rsrc_t rkA = mk_rsrc(p.kA, (size_t)blockIdx.x << 20, 1 << 20);
    const rsrc_t rvA = mk_rsrc(p.vA, (size_t)blockIdx.x << 20, 1 << 20);
    const int lin = lane * 16 + w * 8192;
    const int frg = (4 * (c & 3) + (c >> 2)) * 256 + g * 16 + w * 8192;
    auto ldK_A = [&](int n, int j) -> v4u {
      if (A_VALU) return ld_nt(rkA, lin + 1024 * j, 32768 * n);
      else return ld_nt(rkA, frg + 4096 * (j >> 2) + 64 * (j & 3), 32768 * n);
    };
    auto ldV_A = [&](int n, int j) -> v4u { return ld_nt(rvA, lin + 1024 * j, 32768 * n); };
    v4u K[8], V[8];
    __builtin_amdgcn_s_sleep(8);
#pragma unroll
    for (int j = 0; j < 8; ++j) { wait_vm<WIN0 - 1>(); K[j] = ldK_A(0, j); __builtin_amdgcn_sched_barrier(0); }
#pragma unroll
    for (int j = 0; j < 8; ++j) { wait_vm<WIN0 - 1>(); V[j] = ldV_A(0, j); __builtin_amdgcn_sched_barrier(0); }
    PIN();
    STAMP(2);
    lds_wait(flag + 0);
    v4u QA[4];
    if (A_VALU) { QA[0] = *(const v4u*)(ldu + L_QA + 4 * c); QA[1] = QA[0]; QA[2] = QA[0]; QA[3] = QA[0]; }
    else {
#pragma unroll
      for (int s = 0; s < 4; ++s) QA[s] = *(const v4u*)(ldu + L_QA + 4 * (4 * s + g));
    }
    const unsigned T = (unsigned)__builtin_amdgcn_readfirstlane(ldu[L_T]);
    STAMP(3);
    float mA = -INFINITY, lA = 0.f;
    f32x4 accA[8];
#pragma unroll
    for (int e = 0; e < 8; ++e) accA[e] = f32x4{0.f, 0.f, 0.f, 0.f};
    auto runA = [&](int n0, int n1) {
      const bool kp = (int)blockIdx.x >= KA_LO && (int)blockIdx.x < KA_HI, vp = (int)blockIdx.x >= VA_LO && (int)blockIdx.x < VA_HI;
      if (KA_HI > KA_LO && kp) {
        for (int n = n0; n < n1; ++n)
          step<0, true, true>(K, V, QA, mA, lA, accA, pc, [&](int j) { return ld_pol<0>(rkA, lin + 1024 * j, 32768 * (n + 1)); }, [&](int j) { return ldV_A(n + 1, j); });
      } else if (VA_HI > VA_LO && vp) {
        for (int n = n0; n < n1; ++n)
          step<0, true, true>(K, V, QA, mA, lA, accA, pc, [&](int j) { return ldK_A(n + 1, j); }, [&](int j) { return ld_pol<0>(rvA, lin + 1024 * j, 32768 * (n + 1)); });
      } else
      for (int n = n0; n < n1; ++n)
        step<A_VALU ? 0 : 1, true, true>(K, V, QA, mA, lA, accA, pc, [&](int j) { return ldK_A(n + 1, j); }, [&](int j) { return ldV_A(n + 1, j); });
    };
    runA(0, NA1 - 1);
    {
      const rsrc_t rkB = mk_rsrc(p.kB, (size_t)blockIdx.x << 17, 1 << 17);
      const rsrc_t rvB = mk_rsrc(p.vB, (size_t)blockIdx.x << 17, 1 << 17);
      auto ldK_B = [&](int n, int j) -> v4u { return ld_pol<POL_B>(rkB, frg + 4096 * (j >> 2) + 64 * (j & 3), 32768 * n); };
      auto ldV_B = [&](int n, int j) -> v4u { return ld_pol<POL_B>(rvB, lin + 1024 * j, 32768 * n); };
      step<A_VALU ? 0 : 1, true, true>(K, V, QA, mA, lA, accA, pc, [&](int j) { return ldK_B(0, j); }, [&](int j) { return ldV_B(0, j); });
      STAMP(4);
      lds_wait(flag + 1);
      v4u QB[4];
#pragma unroll
      for (int s = 0; s < 4; ++s) QB[s] = *(const v4u*)(ldu + L_QB + 64 * c7 + 4 * (4 * s + g));
      STAMP(5);
      float mB = -INFINITY, lB = 0.f;
      f32x4 accB[8];
#pragma unroll
      for (int e = 0; e < 8; ++e) accB[e] = f32x4{0.f, 0.f, 0.f, 0.f};
      for (int n = 0; n < 3; ++n)
        step<1, true, true>(K, V, QB, mB, lB, accB, pc, [&](int j) { return ldK_B(n + 1, j); }, [&](int j) { return ldV_B(n + 1, j); });
      step<1, true, true>(K, V, QB, mB, lB, accB, pc, [&](int j) { return ldK_A(NA1, j); }, [&](int j) { return ldV_A(NA1, j); });
      STAMP(6);
      lB = xrow_sum(lB);
      if (c < 8) {
        tile_to_lds(lds + L_RB + (w * 8 + c) * RSTR + 32 * g, accB);
        if (g == 0) *(f32x2*)(lds + L_ML + (w * 8 + c) * 2) = f32x2{mB, lB};
      }
      lds_post(flag + 4 + w);
    }
    STAMP(7);
    runA(NA1, 31);
    STAMP(10);
    {
      const rsrc_t roa = mk_rsrc(p.woA, (size_t)(128 * h) * 2048 + 256 * b, 127 * 2048 + 256);
      const rsrc_t rob = mk_rsrc(p.woB, (size_t)(128 * h) * 2048 + 256 * b, 127 * 2048 + 256);
      const int vo = (32 * w + 8 * g) * 2048 + 16 * c;
      v4u W[8];
#pragma unroll
      for (int j = 0; j < 8; ++j) W[j] = ld_pol<POL_W>(rob, vo, 2048 * j);
      PIN();
      step<A_VALU ? 0 : 1, false, false>(K, V, QA, mA, lA, accA, pc, [&](int j) { return K[j]; }, [&](int j) { return V[j]; });
      PIN();
#pragma unroll
      for (int j = 0; j < 8; ++j) { V[j] = ld_pol<POL_W>(roa, vo, 2048 * j); K[j] = W[j]; }
      PIN();
    }
    STAMP(11);
    const int rbt = tid >> 5, rn4 = 4 * (tid & 31);
    {
      lA = xrow_sum(lA);
      if (c == 0) {
        tile_to_lds(lds + L_RA + w * RSTR + 32 * g, accA);
        if (g == 0) *(f32x2*)(lds + L_ML + 64 + w * 2) = f32x2{mA, lA};
      }
      wg_barrier();
      if (w == 0) {
        f32x2 ml[4];
#pragma unroll
        for (int ww = 0; ww < 4; ++ww) ml[ww] = *(const f32x2*)(lds + L_ML + 64 + ww * 2);
        const float M = fmaxf(fmaxf(ml[0].x, ml[1].x), fmaxf(ml[2].x, ml[3].x));
        f32x4 sum = {0.f, 0.f, 0.f, 0.f};
        float L = 0.f;
#pragma unroll
        for (int ww = 0; ww < 4; ++ww) {
          const float wt = __builtin_amdgcn_exp2f(ml[ww].x - M);
          L = fmaf(wt, ml[ww].y, L);
          sum += wt * *(const f32x4*)(lds + L_RA + ww * RSTR + rn4);
        }
        if (lane < 32) st_sc1(rws, WS_O + ((h * 8 + b) * 128 + rn4) * 4, tag_bf16(sum * (1.f / L), T));
      }
    }
    STAMP(12);
    {
      f32x4 y[8];
      const f32x2 gt = *(const f32x2*)(lds + L_G + 2 * c7);
      {
        const v4u oB = *(const v4u*)(ldu + L_OB + 64 * c7 + 16 * w + 4 * g);
        v4u a[8];
#pragma unroll
        for (int q = 0; q < 4; ++q) tr_pair(K[2 * q], K[2 * q + 1], a, q);
#pragma unroll
        for (int e = 0; e < 8; ++e) y[e] = gt.y * mfma(a[e], oB, f32x4{0.f, 0.f, 0.f, 0.f});
      }
      v4u a[8];
#pragma unroll
      for (int q = 0; q < 4; ++q) tr_pair(V[2 * q], V[2 * q + 1], a, q);
      {
        v4u u[2];
        const int oa = WS_O + ((h * 8 + c7) * 128 + 32 * w + 8 * g) * 4;
        poll<2>(rws, [&](int k) { return oa + 16 * k; }, u, T);
        const v4u oA = pack_hi(u[0], u[1]);
#pragma unroll
        for (int e = 0; e < 8; ++e) y[e] += gt.x * mfma(a[e], oA, f32x4{0.f, 0.f, 0.f, 0.f});
      }
      STAMP(13);
      if (c < 8) tile_to_lds(lds + L_RY + (w * 8 + c) * RSTR + 32 * g, y);
      wg_barrier();
      f32x4 sum = *(const f32x4*)(lds + L_RY + rbt * RSTR + rn4);
#pragma unroll
      for (int ww = 1; ww < 4; ++ww) sum += *(const f32x4*)(lds + L_RY + (ww * 8 + rbt) * RSTR + rn4);
      st_sc1(rws, WS_Y + (((b * 32 + (tid >> 3)) * 32 + h) * 32 + 4 * (tid & 7)) * 4, tag_f32(sum, T));
    }
    STAMP(14);
    if (w == 0) {
      v4u u[4];
      const int yb = WS_Y + (b * 32 + h) * 4096 + lane * 16;
      poll<4>(rws, [&](int k) { return yb + 1024 * k; }, u, T);
      f32x4 sum = (untag(u[0]) + untag(u[1])) + (untag(u[2]) + untag(u[3]));
#pragma unroll
      for (int i = 0; i < 4; ++i) sum[i] = xrow_sum(sum[i] + dpp<0x128>(sum[i]));
      const rsrc_t ro = mk_rsrc(p.out, 0, 8 * 1024 * 2);
      if (lane < 8) {
        const v2u o = {pk_bf16(sum[0], sum[1]), pk_bf16(sum[2], sum[3])};
        __builtin_amdgcn_raw_buffer_store_b64(o, ro, ((h >> 2) * 1024 + 128 * b + 32 * (h & 3) + 4 * lane) * 2, 0, 0);
      }
      if (blockIdx.x == 0 && lane == 0) __builtin_amdgcn_raw_buffer_store_b32(T, rws, WS_EPOCH, 0, AUX_SC1);
    }
    STAMP(15);
  }
}

extern "C" void kernel_launch(void* const* d_in, const int* in_sizes, int n_in, void* d_out, int out_size,
                              void* d_ws, size_t ws_size, hipStream_t stream) {
  Params p;
  p.x = d_in[0]; p.kA = d_in[1]; p.vA = d_in[2]; p.wqA = d_in[3]; p.woA = d_in[4];
  p.kB = d_in[6]; p.vB = d_in[7]; p.wqB = d_in[8]; p.woB = d_in[9]; p.gate = d_in[10];
  p.out = d_out; p.ws = d_ws;
  hipLaunchKernelGGL(fused_decode, dim3(256), dim3(384), 0, stream, p);
}
```

```cpp
#define DIAG 1
#include <hip/hip_runtime.h>
#include <stdint.h>

#define A_VALU 1
#define WIN 6
#define ROUND_S 1
#define NA1 4
#define WIN0 4
#define PACE 0
#define PACE0 (5 * PACE)

typedef unsigned v4u __attribute__((ext_vector_type(4)));
typedef unsigned v2u __attribute__((ext_vector_type(2)));
typedef float f32x4 __attribute__((ext_vector_type(4)));
typedef float f32x2 __attribute__((ext_vector_type(2)));
typedef __bf16 bf16x8 __attribute__((ext_vector_type(8)));
typedef __bf16 bf16x2 __attribute__((ext_vector_type(2)));
typedef __amdgpu_buffer_rsrc_t rsrc_t;

constexpr int AUX_NT = 2, AUX_SC1 = 16;
#define ST_AUX 16
#define LD_AUX 16
#define QDELAY 0
#define PSLEEP 2
#define POL_B 2
#define POL_W 2
#define KA_LO 0
#define KA_HI 0
#define VA_LO 0
#define VA_HI 0
#define WSOFF 0
constexpr int WS_EPOCH = 0;
constexpr int WS_QPART = 4096;
constexpr int WS_QB = WS_QPART + 2 * 32 * 8 * 8 * 128 * 4;
constexpr int WS_BACC = WS_QB + 8 * 32 * 128 * 4;
constexpr int WS_BML = WS_BACC + 8 * 32 * 8 * 128 * 4;
constexpr int WS_O = WS_BML + 8 * 32 * 8 * 4 * 4;
constexpr int WS_G = WS_O + 2 * 32 * 8 * 128 * 4;
constexpr int WS_Y = WS_G + 32 * 8 * 4 * 4;
constexpr int WS_END = WS_Y + 8 * 32 * 32 * 32 * 4;
constexpr int RSTR = 132;
constexpr unsigned SPIN_CAP = 1u << 20;

#define STAMP(i) do {} while (0)

struct Params {
  const void* x; const void* kA; const void* vA; const void* wqA; const void* woA;
  const void* kB; const void* vB; const void* wqB; const void* woB; const void* gate;
  void* out; void* ws;
};

__device__ __forceinline__ rsrc_t mk_rsrc(const void* p, size_t off, int bytes) {
  return __builtin_amdgcn_make_buffer_rsrc((void*)((const char*)p + off), 0, bytes, 0x00020000);
}
__device__ __forceinline__ v4u ld_nt(rsrc_t r, int voff, int soff) { return __builtin_amdgcn_raw_buffer_load_b128(r, voff, soff, AUX_NT); }
template <int AUX> __device__ __forceinline__ v4u ld_pol(rsrc_t r, int voff, int soff) { return __builtin_amdgcn_raw_buffer_load_b128(r, voff, soff, AUX); }
__device__ __forceinline__ v4u ld_plain(rsrc_t r, int voff, int soff) { return __builtin_amdgcn_raw_buffer_load_b128(r, voff, soff, 0); }
__device__ __forceinline__ v4u ld_sc1(rsrc_t r, int voff) { return __builtin_amdgcn_raw_buffer_load_b128(r, voff, 0, LD_AUX); }
__device__ __forceinline__ void st_sc1(rsrc_t r, int voff, v4u v) { __builtin_amdgcn_raw_buffer_store_b128(v, r, voff, 0, ST_AUX); }
template <int N> __device__ __forceinline__ void wait_vm() {
  __builtin_amdgcn_s_waitcnt((N & 15) | ((N >> 4) << 14) | (7 << 4) | (15 << 8));
}
__device__ __forceinline__ void wg_barrier() {
  asm volatile("s_waitcnt lgkmcnt(0)\n\ts_barrier" ::: "memory");
}
#define PIN() do { asm volatile("" ::: "memory"); __builtin_amdgcn_sched_barrier(0); } while (0)

__device__ __forceinline__ float u2f(unsigned u) { return __builtin_bit_cast(float, u); }
__device__ __forceinline__ unsigned f2u(float f) { return __builtin_bit_cast(unsigned, f); }
__device__ __forceinline__ unsigned pk_bf16(float lo, float hi) {
  f32x2 v = {lo, hi};
  return __builtin_bit_cast(unsigned, __builtin_convertvector(v, bf16x2));
}
__device__ __forceinline__ float rnd_bf16(float x) { return u2f(pk_bf16(x, x) & 0xffff0000u); }
__device__ __forceinline__ f32x4 mfma(v4u a, v4u b, f32x4 c) {
  return __builtin_amdgcn_mfma_f32_16x16x32_bf16(__builtin_bit_cast(bf16x8, a), __builtin_bit_cast(bf16x8, b), c, 0, 0, 0);
}
__device__ __forceinline__ float dot2(unsigned a, unsigned b, float c) {
  return __builtin_amdgcn_fdot2_f32_bf16(__builtin_bit_cast(bf16x2, a), __builtin_bit_cast(bf16x2, b), c, false);
}
template <int CTRL> __device__ __forceinline__ float dpp(float x) {
  return u2f((unsigned)__builtin_amdgcn_mov_dpp((int)f2u(x), CTRL, 0xf, 0xf, true));
}
__device__ __forceinline__ float row_allsum(float x) {
  x += dpp<0x128>(x); x += dpp<0x124>(x); x += dpp<0x122>(x); x += dpp<0x121>(x); return x;
}
__device__ __forceinline__ float xrow_max(float x) {
  auto s = __builtin_amdgcn_permlane16_swap(f2u(x), f2u(x), false, false);
  x = fmaxf(u2f(s[0]), u2f(s[1]));
  auto t = __builtin_amdgcn_permlane32_swap(f2u(x), f2u(x), false, false);
  return fmaxf(u2f(t[0]), u2f(t[1]));
}
__device__ __forceinline__ float xrow_sum(float x) {
  auto s = __builtin_amdgcn_permlane16_swap(f2u(x), f2u(x), false, false);
  x = u2f(s[0]) + u2f(s[1]);
  auto t = __builtin_amdgcn_permlane32_swap(f2u(x), f2u(x), false, false);
  return u2f(t[0]) + u2f(t[1]);
}
__device__ __forceinline__ v4u tag_raw(v4u v, unsigned T) {
  v4u r;
  r.x = (v.x & 0xffffff00u) | (T & 0xffu);
  r.y = (v.y & 0xffffff00u) | ((T >> 8) & 0xffu);
  r.z = (v.z & 0xffffff00u) | ((T >> 16) & 0xffu);
  r.w = (v.w & 0xffffff00u) | (T >> 24);
  return r;
}
__device__ __forceinline__ v4u tag_f32(f32x4 v, unsigned T) {
  v4u u = {f2u(v.x) + 0x80u, f2u(v.y) + 0x80u, f2u(v.z) + 0x80u, f2u(v.w) + 0x80u};
  return tag_raw(u, T);
}
__device__ __forceinline__ v4u tag_bf16(f32x4 v, unsigned T) {
  const unsigned a = pk_bf16(v.x, v.y), b = pk_bf16(v.z, v.w);
  v4u u = {a << 16, a & 0xffff0000u, b << 16, b & 0xffff0000u};
  return tag_raw(u, T);
}
__device__ __forceinline__ bool tag_ok(v4u v, unsigned T) {
  const unsigned lo = __builtin_amdgcn_perm(v.y, v.x, 0x0c0c0400u), hi = __builtin_amdgcn_perm(v.w, v.z, 0x04000c0cu);
  return (lo | hi) == T;
}
__device__ __forceinline__ f32x4 untag(v4u v) {
  f32x4 r = {u2f(v.x & 0xffffff00u), u2f(v.y & 0xffffff00u), u2f(v.z & 0xffffff00u), u2f(v.w & 0xffffff00u)};
  return r;
}
__device__ __forceinline__ v4u pack_hi(v4u a, v4u b) {
  v4u r = {__builtin_amdgcn_perm(a.y, a.x, 0x07060302u), __builtin_amdgcn_perm(a.w, a.z, 0x07060302u),
           __builtin_amdgcn_perm(b.y, b.x, 0x07060302u), __builtin_amdgcn_perm(b.w, b.z, 0x07060302u)};
  return r;
}
template <int N, class F> __device__ __forceinline__ void poll(rsrc_t ws, F off, v4u (&v)[N], unsigned T) {
  for (unsigned spin = 0;; ++spin) {
    asm volatile("" ::: "memory");
#pragma unroll
    for (int k = 0; k < N; ++k) v[k] = ld_sc1(ws, off(k));
    bool ok = true;
#pragma unroll
    for (int k = 0; k < N; ++k) ok &= tag_ok(v[k], T);
    if (__builtin_amdgcn_ballot_w64(!ok) == 0ull || spin > SPIN_CAP) break;
    __builtin_amdgcn_s_sleep(PSLEEP);
  }
}
__device__ __forceinline__ void tr_pair(const v4u& r0, const v4u& r1, v4u (&a)[8], int p) {
#pragma unroll
  for (int d = 0; d < 4; ++d) {
    a[2 * d][p] = __builtin_amdgcn_perm(r1[d], r0[d], 0x05040100u);
    a[2 * d + 1][p] = __builtin_amdgcn_perm(r1[d], r0[d], 0x07060302u);
  }
}
__device__ __forceinline__ void tile_to_lds(float* row, const f32x4 (&acc)[8]) {
#pragma unroll
  for (int i = 0; i < 4; ++i) {
    *(f32x4*)(row + 8 * i) = f32x4{acc[0][i], acc[1][i], acc[2][i], acc[3][i]};
    *(f32x4*)(row + 8 * i + 4) = f32x4{acc[4][i], acc[5][i], acc[6][i], acc[7][i]};
  }
}

constexpr float SC_LOG2 = 0.08838834764831845f * 1.4426950408889634f;

struct Pacer {
  unsigned t0, acc;
  __device__ __forceinline__ void tick() {
    if (PACE > 0) {
      acc += PACE;
      while ((unsigned)__builtin_amdgcn_s_memrealtime() - t0 < (acc >> 8)) __builtin_amdgcn_s_sleep(1);
    }
  }
};

template <int MODE, bool HK, bool HV, class FK, class FV>
__device__ __forceinline__ void step(v4u (&K)[8], v4u (&V)[8], const v4u (&Q)[4], float& m, float& l, f32x4 (&acc)[8], Pacer& pc, FK nk, FV nv) {
  float s[8];
  if constexpr (MODE == 0) {
#pragma unroll
    for (int j = 0; j < 8; ++j) {
      float d = dot2(K[j].x, Q[0].x, 0.f);
      d = dot2(K[j].y, Q[0].y, d); d = dot2(K[j].z, Q[0].z, d); d = dot2(K[j].w, Q[0].w, d);
      asm volatile("" : "+v"(d) :: "memory");
      s[j] = d;
      __builtin_amdgcn_sched_barrier(0);
      if constexpr (HK) { if ((j & 3) == 0) pc.tick(); wait_vm<WIN - 1>(); K[j] = nk(j); }
      __builtin_amdgcn_sched_barrier(0);
    }
#pragma unroll
    for (int j = 0; j < 8; ++j) s[j] = row_allsum(s[j]);
  } else {
#pragma unroll
    for (int t = 0; t < 2; ++t) {
      f32x4 d = {0.f, 0.f, 0.f, 0.f};
#pragma unroll
      for (int k = 0; k < 4; ++k) {
        d = mfma(K[4 * t + k], Q[k], d);
        asm volatile("" : "+v"(d) :: "memory");
        __builtin_amdgcn_sched_barrier(0);
        if constexpr (HK) { if (k == 0) pc.tick(); wait_vm<WIN - 1>(); K[4 * t + k] = nk(4 * t + k); }
        __builtin_amdgcn_sched_barrier(0);
      }
#pragma unroll
      for (int i = 0; i < 4; ++i) s[4 * t + i] = d[i];
    }
  }
#pragma unroll
  for (int j = 0; j < 8; ++j) {
    if (ROUND_S) s[j] = rnd_bf16(rnd_bf16(s[j]) * 0.08838834764831845f) * 1.4426950408889634f;
    else s[j] *= SC_LOG2;
  }
  float mx = fmaxf(fmaxf(fmaxf(s[0], s[1]), fmaxf(s[2], s[3])), fmaxf(fmaxf(s[4], s[5]), fmaxf(s[6], s[7])));
  mx = fmaxf(m, xrow_max(mx));
  const float alpha = __builtin_amdgcn_exp2f(m - mx);
  m = mx;
  float ps = 0.f;
#pragma unroll
  for (int j = 0; j < 8; ++j) { s[j] = __builtin_amdgcn_exp2f(s[j] - mx); ps += s[j]; }
  l = fmaf(l, alpha, ps);
  v4u P = {pk_bf16(s[0], s[1]), pk_bf16(s[2], s[3]), pk_bf16(s[4], s[5]), pk_bf16(s[6], s[7])};
#pragma unroll
  for (int e = 0; e < 8; ++e) acc[e] *= alpha;
  v4u a[8];
#pragma unroll
  for (int p = 0; p < 4; ++p) {
    tr_pair(V[2 * p], V[2 * p + 1], a, p);
    asm volatile("" : "+v"(a[0][p]), "+v"(a[1][p]), "+v"(a[2][p]), "+v"(a[3][p]), "+v"(a[4][p]), "+v"(a[5][p]), "+v"(a[6][p]), "+v"(a[7][p]) :: "memory");
    __builtin_amdgcn_sched_barrier(0);
    if constexpr (HV) {
      if ((p & 1) == 0) pc.tick();
      wait_vm<WIN - 1>(); V[2 * p] = nv(2 * p);
      __builtin_amdgcn_sched_barrier(0);
      wait_vm<WIN - 1>(); V[2 * p + 1] = nv(2 * p + 1);
    }
    __builtin_amdgcn_sched_barrier(0);
  }
#pragma unroll
  for (int e = 0; e < 8; ++e) acc[e] = mfma(a[e], P, acc[e]);
}

constexpr int L_FLAG = 0;
constexpr int L_T = 16;
constexpr int L_QA = 32;
constexpr int L_QB = L_QA + 64;
constexpr int L_OB = L_QB + 512;
constexpr int L_G = L_OB + 512;
constexpr int L_ML = L_G + 16;
constexpr int L_RB = L_ML + 64 + 16;
constexpr int L_RA = L_RB + 4 * 8 * RSTR;
constexpr int L_RY = L_RA + 4 * RSTR;
constexpr int L_SV = L_RY + 4 * 8 * RSTR;
constexpr int L_END = L_SV + 2 * 8 * RSTR;
constexpr int L_WORDS = 21 * 1024;
static_assert(L_END <= L_WORDS, "lds");

typedef volatile __attribute__((address_space(3))) unsigned lflag_t;
__device__ __forceinline__ void lds_wait(lflag_t* f) {
  for (unsigned spin = 0; __builtin_amdgcn_readfirstlane(*f) == 0u && spin < (SPIN_CAP << 4); ++spin) __builtin_amdgcn_s_sleep(2);
  asm volatile("" ::: "memory");
}
__device__ __forceinline__ void lds_post(lflag_t* f) {
  asm volatile("s_waitcnt lgkmcnt(0)" ::: "memory");
  *f = 1u;
}

__global__ void __launch_bounds__(384, 2) fused_decode(Params p) {
  __shared__ __attribute__((aligned(16))) float lds[L_WORDS];
  const int tid = threadIdx.x, lane = tid & 63, w = __builtin_amdgcn_readfirstlane(tid >> 6);
  const int g = lane >> 4, c = lane & 15, c7 = lane & 7;
  const int b = blockIdx.x >> 5, h = blockIdx.x & 31;
  unsigned* const ldu = (unsigned*)lds;
  lflag_t* const flag = (lflag_t*)lds + L_FLAG;
  const rsrc_t rws = mk_rsrc(p.ws, (size_t)WSOFF, WS_END);
  STAMP(0);
  if (tid < 16) ldu[L_FLAG + tid] = 0u;
  wg_barrier();

  if (w >= 4) {
    __builtin_amdgcn_s_setprio(3);
    const int mat = w - 4;
    const rsrc_t rx = mk_rsrc(p.x, 0, 8 * 1024 * 2);
    if (mat) __builtin_amdgcn_s_sleep(8);
    const unsigned ep = __builtin_amdgcn_raw_buffer_load_b32(rws, WS_EPOCH, 0, AUX_SC1);
    v4u xf[4], W[32];
#pragma unroll
    for (int kb = 0; kb < 4; ++kb) xf[kb] = ld_plain(rx, c7 * 2048 + (128 * b + 32 * kb + 8 * g) * 2, 0);
    {
      const rsrc_t rq = mk_rsrc(mat ? p.wqB : p.wqA, (size_t)(128 * b) * 8192 + 256 * h, 127 * 8192 + 256);
      const int vo = 8 * g * 8192 + 16 * c;
#pragma unroll
      for (int j = 0; j < 32; ++j) W[j] = ld_pol<POL_W>(rq, vo, 8192 * (32 * (j >> 3) + (j & 7)));
      PIN();
    }
    const unsigned T = (unsigned)__builtin_amdgcn_readfirstlane(ep) + 0x9E3779B1u;
    {
      f32x4 d[8];
#pragma unroll
      for (int e = 0; e < 8; ++e) d[e] = f32x4{0.f, 0.f, 0.f, 0.f};
#pragma unroll
      for (int kb = 0; kb < 4; ++kb) {
        v4u a[8];
#pragma unroll
        for (int q = 0; q < 4; ++q) tr_pair(W[8 * kb + 2 * q], W[8 * kb + 2 * q + 1], a, q);
#pragma unroll
        for (int e = 0; e < 8; ++e) d[e] = mfma(a[e], xf[kb], d[e]);
      }
      STAMP(1);
      float* sv = lds + L_SV + mat * 8 * RSTR;
      if (c < 8) tile_to_lds(sv + c * RSTR + 32 * g, d);
      asm volatile("s_waitcnt lgkmcnt(0)" ::: "memory");
#pragma unroll
      for (int k = 0; k < 4; ++k) {
        const int bt = 2 * k + (lane >> 5), n4 = 4 * (lane & 31);
        st_sc1(rws, WS_QPART + ((((mat * 32 + h) * 8 + bt) * 8 + b) * 128 + n4) * 4, tag_f32(*(const f32x4*)(sv + bt * RSTR + n4), T));
      }
    }
    STAMP(2);
    asm volatile("s_waitcnt vmcnt(0)" ::: "memory");
    STAMP(8);
    if (QDELAY > 0) { const unsigned tq = (unsigned)__builtin_amdgcn_s_memrealtime(); while ((unsigned)__builtin_amdgcn_s_memrealtime() - tq < QDELAY) __builtin_amdgcn_s_sleep(2); }
    float q[8];
    {
      v4u u[4];
      const int base = WS_QPART + (((mat * 32 + h) * 8 + b) * 1024 + g * 128 + 8 * c) * 4;
      poll<4>(rws, [&](int k) { return base + (k >> 1) * 2048 + (k & 1) * 16; }, u, T);
      const f32x4 lo = untag(u[0]) + untag(u[2]), hi = untag(u[1]) + untag(u[3]);
#pragma unroll
      for (int i = 0; i < 4; ++i) { q[i] = xrow_sum(lo[i]); q[4 + i] = xrow_sum(hi[i]); }
    }
    if (mat == 0) {
      const v4u qq = {pk_bf16(q[0], q[1]), pk_bf16(q[2], q[3]), pk_bf16(q[4], q[5]), pk_bf16(q[6], q[7])};
      if (g == 0) *(v4u*)(ldu + L_QA + 4 * c) = qq;
      if (lane == 0) ldu[L_T] = T;
      lds_post(flag + 0);
      STAMP(3);
      const rsrc_t roa = mk_rsrc(p.woA, (size_t)(128 * h) * 2048 + 256 * b, 64);
      const rsrc_t rob = mk_rsrc(p.woB, (size_t)(128 * h) * 2048 + 256 * b, 64);
      const rsrc_t ro = mk_rsrc(p.out, 0, 64);
      unsigned t0 = __builtin_amdgcn_raw_buffer_load_b32(roa, 0, 0, AUX_NT);
      unsigned t1 = __builtin_amdgcn_raw_buffer_load_b32(rob, 0, 0, AUX_NT);
      unsigned t2 = __builtin_amdgcn_raw_buffer_load_b32(ro, 0, 0, AUX_NT);
      asm volatile("" :: "v"(t0), "v"(t1), "v"(t2));
    } else {
      {
        const f32x4 v = (g & 1) ? f32x4{q[4], q[5], q[6], q[7]} : f32x4{q[0], q[1], q[2], q[3]};
        if (g < 2) st_sc1(rws, WS_QB + ((b * 32 + h) * 128 + 8 * c + 4 * g) * 4, tag_bf16(v, T));
      }
      float gt;
      {
        const rsrc_t rg = mk_rsrc(p.gate, 0, 2048);
        const v4u x0 = ld_plain(rx, b * 2048 + lane * 32, 0), x1 = ld_plain(rx, b * 2048 + lane * 32 + 16, 0);
        const v4u g0 = ld_plain(rg, lane * 32, 0), g1 = ld_plain(rg, lane * 32 + 16, 0);
        float gs = 0.f;
#pragma unroll
        for (int i = 0; i < 4; ++i) {
          gs += rnd_bf16(u2f(x0[i] << 16) * u2f(g0[i] << 16)) + rnd_bf16(u2f(x0[i] & 0xffff0000u) * u2f(g0[i] & 0xffff0000u));
          gs += rnd_bf16(u2f(x1[i] << 16) * u2f(g1[i] << 16)) + rnd_bf16(u2f(x1[i] & 0xffff0000u) * u2f(g1[i] & 0xffff0000u));
        }
        gs = rnd_bf16(xrow_sum(row_allsum(gs)));
        gt = rnd_bf16(1.f / (1.f + __expf(-gs)));
      }
      if (lane == 0) st_sc1(rws, WS_G + (h * 8 + b) * 16, tag_bf16(f32x4{gt, 1.f - gt, 0.f, 0.f}, T));
      {
        v4u u[4];
        const int base = WS_QB + (b * 32 + (h & 24)) * 512 + lane * 16;
        poll<4>(rws, [&](int k) { return base + 1024 * k; }, u, T);
#pragma unroll
        for (int k = 0; k < 4; ++k)
          *(v2u*)(ldu + L_QB + 128 * k + 2 * lane) = v2u{__builtin_amdgcn_perm(u[k].y, u[k].x, 0x07060302u), __builtin_amdgcn_perm(u[k].w, u[k].z, 0x07060302u)};
        lds_post(flag + 1);
      }
      STAMP(3);
      lds_wait(flag + 4); lds_wait(flag + 5); lds_wait(flag + 6); lds_wait(flag + 7);
      STAMP(4);
#pragma unroll
      for (int k = 0; k < 4; ++k) {
        const int hd = 2 * k + (lane >> 5), n4 = 4 * (lane & 31);
        f32x2 ml[4];
#pragma unroll
        for (int ww = 0; ww < 4; ++ww) ml[ww] = *(const f32x2*)(lds + L_ML + (ww * 8 + hd) * 2);
        const float M = fmaxf(fmaxf(ml[0].x, ml[1].x), fmaxf(ml[2].x, ml[3].x));
        f32x4 sum = {0.f, 0.f, 0.f, 0.f};
        float L = 0.f;
#pragma unroll
        for (int ww = 0; ww < 4; ++ww) {
          const float wt = __builtin_amdgcn_exp2f(ml[ww].x - M);
          L = fmaf(wt, ml[ww].y, L);
          sum += wt * *(const f32x4*)(lds + L_RB + (ww * 8 + hd) * RSTR + n4);
        }
        const int rec = (b * 32 + (h & 24) + hd) * 8 + (h & 7);
        st_sc1(rws, WS_BACC + (rec * 128 + n4) * 4, tag_f32(sum, T));
        if (n4 == 0) {
          const float Mh = u2f(f2u(M) & 0xffffff00u);
          const v4u r = {f2u(Mh), f2u(M - Mh), f2u(L) + 0x80u, 0u};
          st_sc1(rws, WS_BML + rec * 16, tag_raw(r, T));
        }
      }
      {
        v4u u[5];
        const int rec = (b * 32 + h) * 8;
        const int hf = lane >> 5;
        poll<5>(rws, [&](int k) { return k < 4 ? WS_BACC + ((rec + 2 * k + hf) * 128 + 4 * (lane & 31)) * 4 : WS_BML + (rec + c7) * 16; }, u, T);
        const f32x4 r = untag(u[4]);
        const float ms = r.x + r.y;
        float M = fmaxf(ms, dpp<0x124>(ms)); M = fmaxf(M, dpp<0x122>(M)); M = fmaxf(M, dpp<0x121>(M));
        const float wt = __builtin_amdgcn_exp2f(ms - M);
        float L = wt * r.z; L += dpp<0x124>(L); L += dpp<0x122>(L); L += dpp<0x121>(L);
        f32x4 sum = {0.f, 0.f, 0.f, 0.f};
#pragma unroll
        for (int k = 0; k < 4; ++k) sum += u2f((unsigned)__builtin_amdgcn_ds_bpermute((2 * k + hf) * 4, (int)f2u(wt))) * untag(u[k]);
#pragma unroll
        for (int i = 0; i < 4; ++i) {
          auto t = __builtin_amdgcn_permlane32_swap(f2u(sum[i]), f2u(sum[i]), false, false);
          sum[i] = u2f(t[0]) + u2f(t[1]);
        }
        if (lane < 32) st_sc1(rws, WS_O + (((32 + h) * 8 + b) * 128 + 4 * lane) * 4, tag_bf16(sum * (1.f / L), T));
      }
      STAMP(5);
      {
        v4u u[5];
        const int base = WS_O + (32 + h) * 4096 + lane * 16;
        poll<5>(rws, [&](int k) { return k < 4 ? base + 1024 * k : WS_G + (h * 8 + c7) * 16; }, u, T);
#pragma unroll
        for (int k = 0; k < 4; ++k)
          *(v2u*)(ldu + L_OB + 128 * k + 2 * lane) = v2u{__builtin_amdgcn_perm(u[k].y, u[k].x, 0x07060302u), __builtin_amdgcn_perm(u[k].w, u[k].z, 0x07060302u)};
        if (lane < 8) *(v2u*)(ldu + L_G + 2 * lane) = v2u{u[4].x & 0xffff0000u, u[4].y & 0xffff0000u};
        asm volatile("s_waitcnt lgkmcnt(0)" ::: "memory");
      }
      STAMP(6);
    }
  } else {
    Pacer pc;
    pc.t0 = (unsigned)__builtin_amdgcn_s_memrealtime();
    pc.acc = PACE0;
    const rsrc_t rkA = mk_rsrc(p.kA, (size_t)blockIdx.x << 20, 1 << 20);
    const rsrc_t rvA = mk_rsrc(p.vA, (size_t)blockIdx.x << 20, 1 << 20);
    const int lin = lane * 16 + w * 8192;
    const int frg = (4 * (c & 3) + (c >> 2)) * 256 + g * 16 + w * 8192;
    auto ldK_A = [&](int n, int j) -> v4u {
      if (A_VALU) return ld_nt(rkA, lin + 1024 * j, 32768 * n);
      else return ld_nt(rkA, frg + 4096 * (j >> 2) + 64 * (j & 3), 32768 * n);
    };
    auto ldV_A = [&](int n, int j) -> v4u { return ld_nt(rvA, lin + 1024 * j, 32768 * n); };
    v4u K[8], V[8];
    __builtin_amdgcn_s_sleep(8);
#pragma unroll
    for (int j = 0; j < 8; ++j) { wait_vm<WIN0 - 1>(); K[j] = ldK_A(0, j); __builtin_amdgcn_sched_barrier(0); }
#pragma unroll
    for (int j = 0; j < 8; ++j) { wait_vm<WIN0 - 1>(); V[j] = ldV_A(0, j); __builtin_amdgcn_sched_barrier(0); }
    PIN();
    STAMP(2);
    const unsigned dg2 = (unsigned)__builtin_amdgcn_s_memrealtime();
    lds_wait(flag + 0);
    v4u QA[4];
    if (A_VALU) { QA[0] = *(const v4u*)(ldu + L_QA + 4 * c); QA[1] = QA[0]; QA[2] = QA[0]; QA[3] = QA[0]; }
    else {
#pragma unroll
      for (int s = 0; s < 4; ++s) QA[s] = *(const v4u*)(ldu + L_QA + 4 * (4 * s + g));
    }
    const unsigned T = (unsigned)__builtin_amdgcn_readfirstlane(ldu[L_T]);
    STAMP(3);
    const unsigned dg3 = (unsigned)__builtin_amdgcn_s_memrealtime();
    float mA = -INFINITY, lA = 0.f;
    f32x4 accA[8];
#pragma unroll
    for (int e = 0; e < 8; ++e) accA[e] = f32x4{0.f, 0.f, 0.f, 0.f};
    auto runA = [&](int n0, int n1) {
      const bool kp = (int)blockIdx.x >= KA_LO && (int)blockIdx.x < KA_HI, vp = (int)blockIdx.x >= VA_LO && (int)blockIdx.x < VA_HI;
      if (KA_HI > KA_LO && kp) {
        for (int n = n0; n < n1; ++n)
          step<0, true, true>(K, V, QA, mA, lA, accA, pc, [&](int j) { return ld_pol<0>(rkA, lin + 1024 * j, 32768 * (n + 1)); }, [&](int j) { return ldV_A(n + 1, j); });
      } else if (VA_HI > VA_LO && vp) {
        for (int n = n0; n < n1; ++n)
          step<0, true, true>(K, V, QA, mA, lA, accA, pc, [&](int j) { return ldK_A(n + 1, j); }, [&](int j) { return ld_pol<0>(rvA, lin + 1024 * j, 32768 * (n + 1)); });
      } else
      for (int n = n0; n < n1; ++n)
        step<A_VALU ? 0 : 1, true, true>(K, V, QA, mA, lA, accA, pc, [&](int j) { return ldK_A(n + 1, j); }, [&](int j) { return ldV_A(n + 1, j); });
    };
    runA(0, NA1 - 1);
    {
      const rsrc_t rkB = mk_rsrc(p.kB, (size_t)blockIdx.x << 17, 1 << 17);
      const rsrc_t rvB = mk_rsrc(p.vB, (size_t)blockIdx.x << 17, 1 << 17);
      auto ldK_B = [&](int n, int j) -> v4u { return ld_pol<POL_B>(rkB, frg + 4096 * (j >> 2) + 64 * (j & 3), 32768 * n); };
      auto ldV_B = [&](int n, int j) -> v4u { return ld_pol<POL_B>(rvB, lin + 1024 * j, 32768 * n); };
      step<A_VALU ? 0 : 1, true, true>(K, V, QA, mA, lA, accA, pc, [&](int j) { return ldK_B(0, j); }, [&](int j) { return ldV_B(0, j); });
      STAMP(4);
      lds_wait(flag + 1);
      v4u QB[4];
#pragma unroll
      for (int s = 0; s < 4; ++s) QB[s] = *(const v4u*)(ldu + L_QB + 64 * c7 + 4 * (4 * s + g));
      STAMP(5);
      float mB = -INFINITY, lB = 0.f;
      f32x4 accB[8];
#pragma unroll
      for (int e = 0; e < 8; ++e) accB[e] = f32x4{0.f, 0.f, 0.f, 0.f};
      for (int n = 0; n < 3; ++n)
        step<1, true, true>(K, V, QB, mB, lB, accB, pc, [&](int j) { return ldK_B(n + 1, j); }, [&](int j) { return ldV_B(n + 1, j); });
      step<1, true, true>(K, V, QB, mB, lB, accB, pc, [&](int j) { return ldK_A(NA1, j); }, [&](int j) { return ldV_A(NA1, j); });
      STAMP(6);
      lB = xrow_sum(lB);
      if (c < 8) {
        tile_to_lds(lds + L_RB + (w * 8 + c) * RSTR + 32 * g, accB);
        if (g == 0) *(f32x2*)(lds + L_ML + (w * 8 + c) * 2) = f32x2{mB, lB};
      }
      lds_post(flag + 4 + w);
    }
    STAMP(7);
    runA(NA1, 31);
    STAMP(10);
    const unsigned dg10 = (unsigned)__builtin_amdgcn_s_memrealtime();
    {
      const rsrc_t roa = mk_rsrc(p.woA, (size_t)(128 * h) * 2048 + 256 * b, 127 * 2048 + 256);
      const rsrc_t rob = mk_rsrc(p.woB, (size_t)(128 * h) * 2048 + 256 * b, 127 * 2048 + 256);
      const int vo = (32 * w + 8 * g) * 2048 + 16 * c;
      v4u W[8];
#pragma unroll
      for (int j = 0; j < 8; ++j) W[j] = ld_pol<POL_W>(rob, vo, 2048 * j);
      PIN();
      step<A_VALU ? 0 : 1, false, false>(K, V, QA, mA, lA, accA, pc, [&](int j) { return K[j]; }, [&](int j) { return V[j]; });
      PIN();
#pragma unroll
      for (int j = 0; j < 8; ++j) { V[j] = ld_pol<POL_W>(roa, vo, 2048 * j); K[j] = W[j]; }
      PIN();
    }
    STAMP(11);
    const int rbt = tid >> 5, rn4 = 4 * (tid & 31);
    {
      lA = xrow_sum(lA);
      if (c == 0) {
        tile_to_lds(lds + L_RA + w * RSTR + 32 * g, accA);
        if (g == 0) *(f32x2*)(lds + L_ML + 64 + w * 2) = f32x2{mA, lA};
      }
      wg_barrier();
      if (w == 0) {
        f32x2 ml[4];
#pragma unroll
        for (int ww = 0; ww < 4; ++ww) ml[ww] = *(const f32x2*)(lds + L_ML + 64 + ww * 2);
        const float M = fmaxf(fmaxf(ml[0].x, ml[1].x), fmaxf(ml[2].x, ml[3].x));
        f32x4 sum = {0.f, 0.f, 0.f, 0.f};
        float L = 0.f;
#pragma unroll
        for (int ww = 0; ww < 4; ++ww) {
          const float wt = __builtin_amdgcn_exp2f(ml[ww].x - M);
          L = fmaf(wt, ml[ww].y, L);
          sum += wt * *(const f32x4*)(lds + L_RA + ww * RSTR + rn4);
        }
        if (lane < 32) st_sc1(rws, WS_O + ((h * 8 + b) * 128 + rn4) * 4, tag_bf16(sum * (1.f / L), T));
      }
    }
    STAMP(12);
    {
      f32x4 y[8];
      const f32x2 gt = *(const f32x2*)(lds + L_G + 2 * c7);
      {
        const v4u oB = *(const v4u*)(ldu + L_OB + 64 * c7 + 16 * w + 4 * g);
        v4u a[8];
#pragma unroll
        for (int q = 0; q < 4; ++q) tr_pair(K[2 * q], K[2 * q + 1], a, q);
#pragma unroll
        for (int e = 0; e < 8; ++e) y[e] = gt.y * mfma(a[e], oB, f32x4{0.f, 0.f, 0.f, 0.f});
      }
      v4u a[8];
#pragma unroll
      for (int q = 0; q < 4; ++q) tr_pair(V[2 * q], V[2 * q + 1], a, q);
      {
        v4u u[2];
        const int oa = WS_O + ((h * 8 + c7) * 128 + 32 * w + 8 * g) * 4;
        poll<2>(rws, [&](int k) { return oa + 16 * k; }, u, T);
        const v4u oA = pack_hi(u[0], u[1]);
#pragma unroll
        for (int e = 0; e < 8; ++e) y[e] += gt.x * mfma(a[e], oA, f32x4{0.f, 0.f, 0.f, 0.f});
      }
      STAMP(13);
      if (c < 8) tile_to_lds(lds + L_RY + (w * 8 + c) * RSTR + 32 * g, y);
      wg_barrier();
      f32x4 sum = *(const f32x4*)(lds + L_RY + rbt * RSTR + rn4);
#pragma unroll
      for (int ww = 1; ww < 4; ++ww) sum += *(const f32x4*)(lds + L_RY + (ww * 8 + rbt) * RSTR + rn4);
      st_sc1(rws, WS_Y + (((b * 32 + (tid >> 3)) * 32 + h) * 32 + 4 * (tid & 7)) * 4, tag_f32(sum, T));
    }
    STAMP(14);
    if (w == 0) {
      v4u u[4];
      const int yb = WS_Y + (b * 32 + h) * 4096 + lane * 16;
      poll<4>(rws, [&](int k) { return yb + 1024 * k; }, u, T);
      f32x4 sum = (untag(u[0]) + untag(u[1])) + (untag(u[2]) + untag(u[3]));
#pragma unroll
      for (int i = 0; i < 4; ++i) sum[i] = xrow_sum(sum[i] + dpp<0x128>(sum[i]));
      const rsrc_t ro = mk_rsrc(p.out, 0, 8 * 1024 * 2);
      if (lane < 8) {
        const v2u o = {pk_bf16(sum[0], sum[1]), pk_bf16(sum[2], sum[3])};
        __builtin_amdgcn_raw_buffer_store_b64(o, ro, ((h >> 2) * 1024 + 128 * b + 32 * (h & 3) + 4 * lane) * 2, 0, 0);
      }
      if (blockIdx.x == 0 && lane == 0) __builtin_amdgcn_raw_buffer_store_b32(T, rws, WS_EPOCH, 0, AUX_SC1);
    }
    STAMP(15);
    {
      const unsigned now = (unsigned)__builtin_amdgcn_s_memrealtime();
      unsigned until = 15000;
      if (DIAG == 2) until = 15000 + 10 * (dg3 - pc.t0);
      if (DIAG == 3) until = 15000 + (dg10 - pc.t0);
      if (DIAG == 4) until = 15000 + (now - pc.t0);
      if (DIAG == 5) until = blockIdx.x == 77 ? 15000 + (dg10 - pc.t0) : 0;
      if (DIAG == 6) until = 15000 + 10 * (dg2 - pc.t0);
      while ((unsigned)__builtin_amdgcn_s_memrealtime() - pc.t0 < until) __builtin_amdgcn_s_sleep(1);
    }
  }
}

extern "C" void kernel_launch(void* const* d_in, const int* in_sizes, int n_in, void* d_out, int out_size,
                              void* d_ws, size_t ws_size, hipStream_t stream) {
  Params p;
  p.x = d_in[0]; p.kA = d_in[1]; p.vA = d_in[2]; p.wqA = d_in[3]; p.woA = d_in[4];
  p.kB = d_in[6]; p.vB = d_in[7]; p.wqB = d_in[8]; p.woB = d_in[9]; p.gate = d_in[10];
  p.out = d_out; p.ws = d_ws;
  hipLaunchKernelGGL(fused_decode, dim3(256), dim3(384), 0, stream, p);
}
```

```cpp
#define P2 14200
#define PT0 (300*256)
#include <hip/hip_runtime.h>
#include <stdint.h>

#define WIN 8
#define P1 14600
#define NPK 9
#define NPV 17
#define QDELAY 100
#define HX 3
#define FORCE -1

typedef unsigned v4u __attribute__((ext_vector_type(4)));
typedef unsigned v2u __attribute__((ext_vector_type(2)));
typedef float f32x4 __attribute__((ext_vector_type(4)));
typedef float f32x2 __attribute__((ext_vector_type(2)));
typedef __bf16 bf16x8 __attribute__((ext_vector_type(8)));
typedef __bf16 bf16x2 __attribute__((ext_vector_type(2)));
typedef __amdgpu_buffer_rsrc_t rsrc_t;

constexpr int AUX_NT = 2, AUX_SC1 = 16;
constexpr int OOB = 0x40000000;
constexpr int WS_EPOCH = 0;
constexpr int WS_QPART = 4096;
constexpr int WS_QB = WS_QPART + 2 * 32 * 8 * 8 * 128 * 4;
constexpr int WS_BACC = WS_QB + 8 * 32 * 128 * 4;
constexpr int WS_BML = WS_BACC + 8 * 32 * 8 * 128 * 4;
constexpr int WS_O = WS_BML + 8 * 32 * 8 * 4 * 4;
constexpr int WS_G = WS_O + 2 * 32 * 8 * 128 * 4;
constexpr int WS_Y = WS_G + 32 * 8 * 4 * 4;
constexpr int WS_AP = WS_Y + 8 * 32 * 32 * 32 * 4;
constexpr int WS_PR = WS_AP + 256 * 320 * 4;
constexpr int WS_IN = WS_PR + 256 * 4 * 32 * 4;
constexpr int WS_END = WS_IN + 256 * 8192;
constexpr int RSTR = 132;
constexpr unsigned SPIN_CAP = 1u << 20;
constexpr unsigned TAG_INC = 0x9E3579B1u;

#define STAMP(i) do {} while (0)

struct Params {
  const void* x; const void* kA; const void* vA; const void* wqA; const void* woA;
  const void* kB; const void* vB; const void* wqB; const void* woB; const void* gate;
  void* out; void* ws;
};

__device__ __forceinline__ rsrc_t mk_rsrc(const void* p, size_t off, int bytes) {
  return __builtin_amdgcn_make_buffer_rsrc((void*)((const char*)p + off), 0, bytes, 0x00020000);
}
__device__ __forceinline__ v4u ld_nt(rsrc_t r, int voff, int soff) { return __builtin_amdgcn_raw_buffer_load_b128(r, voff, soff, AUX_NT); }
__device__ __forceinline__ v4u ld_plain(rsrc_t r, int voff, int soff) { return __builtin_amdgcn_raw_buffer_load_b128(r, voff, soff, 0); }
__device__ __forceinline__ v4u ld_sc1(rsrc_t r, int voff, int soff = 0) { return __builtin_amdgcn_raw_buffer_load_b128(r, voff, soff, AUX_SC1); }
__device__ __forceinline__ void st_sc1(rsrc_t r, int voff, v4u v, int soff = 0) { __builtin_amdgcn_raw_buffer_store_b128(v, r, voff, soff, AUX_SC1); }
template <int N> __device__ __forceinline__ void wait_vm() {
  __builtin_amdgcn_s_waitcnt((N & 15) | ((N >> 4) << 14) | (7 << 4) | (15 << 8));
}
__device__ __forceinline__ void drain() { asm volatile("s_waitcnt vmcnt(0)" ::: "memory"); }
__device__ __forceinline__ void wg_barrier() { asm volatile("s_waitcnt lgkmcnt(0)\n\ts_barrier" ::: "memory"); }
#define PIN() do { asm volatile("" ::: "memory"); __builtin_amdgcn_sched_barrier(0); } while (0)
#define SB() __builtin_amdgcn_sched_barrier(0)

__device__ __forceinline__ float u2f(unsigned u) { return __builtin_bit_cast(float, u); }
__device__ __forceinline__ unsigned f2u(float f) { return __builtin_bit_cast(unsigned, f); }
__device__ __forceinline__ unsigned pk_bf16(float lo, float hi) {
  f32x2 v = {lo, hi};
  return __builtin_bit_cast(unsigned, __builtin_convertvector(v, bf16x2));
}
__device__ __forceinline__ float rnd_bf16(float x) { return u2f(pk_bf16(x, x) & 0xffff0000u); }
__device__ __forceinline__ f32x4 mfma(v4u a, v4u b, f32x4 c) {
  return __builtin_amdgcn_mfma_f32_16x16x32_bf16(__builtin_bit_cast(bf16x8, a), __builtin_bit_cast(bf16x8, b), c, 0, 0, 0);
}
__device__ __forceinline__ float dot2(unsigned a, unsigned b, float c) {
  return __builtin_amdgcn_fdot2_f32_bf16(__builtin_bit_cast(bf16x2, a), __builtin_bit_cast(bf16x2, b), c, false);
}
template <int CTRL> __device__ __forceinline__ float dpp(float x) {
  return u2f((unsigned)__builtin_amdgcn_mov_dpp((int)f2u(x), CTRL, 0xf, 0xf, true));
}
__device__ __forceinline__ float row_allsum(float x) {
  x += dpp<0x128>(x); x += dpp<0x124>(x); x += dpp<0x122>(x); x += dpp<0x121>(x); return x;
}
__device__ __forceinline__ float xrow_max(float x) {
  auto s = __builtin_amdgcn_permlane16_swap(f2u(x), f2u(x), false, false);
  x = fmaxf(u2f(s[0]), u2f(s[1]));
  auto t = __builtin_amdgcn_permlane32_swap(f2u(x), f2u(x), false, false);
  return fmaxf(u2f(t[0]), u2f(t[1]));
}
__device__ __forceinline__ float half_sum(float x) {
  auto t = __builtin_amdgcn_permlane32_swap(f2u(x), f2u(x), false, false);
  return u2f(t[0]) + u2f(t[1]);
}
__device__ __forceinline__ float xrow_sum(float x) {
  auto s = __builtin_amdgcn_permlane16_swap(f2u(x), f2u(x), false, false);
  return half_sum(u2f(s[0]) + u2f(s[1]));
}
template <unsigned M> __device__ __forceinline__ v4u tag_raw(v4u v, unsigned T) {
  v4u r;
  r.x = (v.x & ~M) | (T & M);
  r.y = (v.y & ~M) | ((T >> 8) & M);
  r.z = (v.z & ~M) | ((T >> 16) & M);
  r.w = (v.w & ~M) | ((T >> 24) & M);
  return r;
}
template <unsigned M> __device__ __forceinline__ v4u tag_f32(f32x4 v, unsigned T) {
  constexpr unsigned H = (M + 1) / 2;
  v4u u = {f2u(v.x) + H, f2u(v.y) + H, f2u(v.z) + H, f2u(v.w) + H};
  return tag_raw<M>(u, T);
}
__device__ __forceinline__ v4u tag_bf16(f32x4 v, unsigned T) {
  const unsigned a = pk_bf16(v.x, v.y), b = pk_bf16(v.z, v.w);
  v4u u = {a << 16, a & 0xffff0000u, b << 16, b & 0xffff0000u};
  return tag_raw<0xffu>(u, T);
}
template <unsigned M> __device__ __forceinline__ bool tag_ok(v4u v, unsigned T) {
  const unsigned lo = __builtin_amdgcn_perm(v.y, v.x, 0x0c0c0400u), hi = __builtin_amdgcn_perm(v.w, v.z, 0x04000c0cu);
  return (((lo | hi) ^ T) & (M * 0x01010101u)) == 0u;
}
template <unsigned M> __device__ __forceinline__ f32x4 untag(v4u v) {
  f32x4 r = {u2f(v.x & ~M), u2f(v.y & ~M), u2f(v.z & ~M), u2f(v.w & ~M)};
  return r;
}
__device__ __forceinline__ v2u pack_hi(v4u a) {
  return v2u{__builtin_amdgcn_perm(a.y, a.x, 0x07060302u), __builtin_amdgcn_perm(a.w, a.z, 0x07060302u)};
}
template <unsigned M, int N, int SLP = 2, class F> __device__ __forceinline__ void poll(rsrc_t ws, F off, v4u (&v)[N], unsigned T) {
  for (unsigned spin = 0;; ++spin) {
    asm volatile("" ::: "memory");
#pragma unroll
    for (int k = 0; k < N; ++k) v[k] = ld_sc1(ws, off(k));
    bool ok = true;
#pragma unroll
    for (int k = 0; k < N; ++k) ok &= tag_ok<M>(v[k], T);
    if (__builtin_amdgcn_ballot_w64(!ok) == 0ull || spin > SPIN_CAP) break;
    __builtin_amdgcn_s_sleep(SLP);
  }
}
__device__ __forceinline__ void tr_pair(const v4u& r0, const v4u& r1, v4u (&a)[8], int p) {
#pragma unroll
  for (int d = 0; d < 4; ++d) {
    a[2 * d][p] = __builtin_amdgcn_perm(r1[d], r0[d], 0x05040100u);
    a[2 * d + 1][p] = __builtin_amdgcn_perm(r1[d], r0[d], 0x07060302u);
  }
}
__device__ __forceinline__ f32x4 tile_vec(const f32x4 (&acc)[8], int k, bool hi) {
  f32x4 r;
#pragma unroll
  for (int e = 0; e < 4; ++e) r[e] = hi ? acc[4 * (k & 1) + e][2 * (k >> 1) + 1] : acc[4 * (k & 1) + e][2 * (k >> 1)];
  return r;
}
__device__ __forceinline__ int tile_n0(int k, int g, int hi) { return 32 * g + 16 * (k >> 1) + 8 * hi + 4 * (k & 1); }

struct Pacer {
  unsigned t0, acc, inc;
  __device__ __forceinline__ void tick() {
    while ((unsigned)__builtin_amdgcn_s_memrealtime() - t0 < (acc >> 8)) __builtin_amdgcn_s_sleep(1);
    acc += inc;
  }
};
__device__ __forceinline__ unsigned round_scores(float a, float b) {
  const unsigned u = pk_bf16(a, b);
  return pk_bf16(u2f(u << 16) * 0.08838834764831845f, u2f(u & 0xffff0000u) * 0.08838834764831845f);
}
template <bool RF, class F>
__device__ __forceinline__ v4u k_part(v4u (&R)[8], const v4u& q, Pacer& pc, F nx) {
  float s[8];
#pragma unroll
  for (int j = 0; j < 8; ++j) {
    float d = dot2(R[j].x, q.x, 0.f);
    d = dot2(R[j].y, q.y, d); d = dot2(R[j].z, q.z, d); d = dot2(R[j].w, q.w, d);
    asm volatile("" : "+v"(d) :: "memory");
    s[j] = d;
    SB();
    if constexpr (RF) { if ((j & 3) == 0) pc.tick(); wait_vm<WIN - 1>(); R[j] = nx(j); }
    SB();
  }
#pragma unroll
  for (int j = 0; j < 8; ++j) s[j] = row_allsum(s[j]);
  return v4u{round_scores(s[0], s[1]), round_scores(s[2], s[3]), round_scores(s[4], s[5]), round_scores(s[6], s[7])};
}
template <class F>
__device__ __forceinline__ v4u k_part8(v4u (&R)[8], const v4u (&Q)[4], unsigned* tile, int g, int c, Pacer& pc, F nx) {
#pragma unroll
  for (int j = 0; j < 8; ++j) {
    *(v4u*)(tile + (4 * j + g) * 64 + (c ^ (4 * g + (j & 3))) * 4) = R[j];
    PIN();
    if ((j & 3) == 0) pc.tick();
    wait_vm<WIN - 1>(); R[j] = nx(j);
    SB();
  }
  f32x4 d[2];
#pragma unroll
  for (int t = 0; t < 2; ++t) {
    d[t] = f32x4{0.f, 0.f, 0.f, 0.f};
#pragma unroll
    for (int k = 0; k < 4; ++k) d[t] = mfma(*(const v4u*)(tile + (16 * t + 4 * (c & 3) + (c >> 2)) * 64 + ((4 * k + g) ^ c) * 4), Q[k], d[t]);
  }
  v4u r = {round_scores(d[0][0], d[0][1]), round_scores(d[0][2], d[0][3]), round_scores(d[1][0], d[1][1]), round_scores(d[1][2], d[1][3])};
  asm volatile("" : "+v"(r) :: "memory");
  return r;
}
template <bool RF, class F>
__device__ __forceinline__ void v_part(v4u (&R)[8], const v4u& P, f32x4 (&acc)[8], Pacer& pc, F nx) {
  v4u a[8];
#pragma unroll
  for (int p = 0; p < 4; ++p) {
    tr_pair(R[2 * p], R[2 * p + 1], a, p);
    asm volatile("" : "+v"(a[0][p]), "+v"(a[1][p]), "+v"(a[2][p]), "+v"(a[3][p]), "+v"(a[4][p]), "+v"(a[5][p]), "+v"(a[6][p]), "+v"(a[7][p]) :: "memory");
    SB();
    if constexpr (RF) {
      if ((p & 1) == 0) pc.tick();
      wait_vm<WIN - 1>(); R[2 * p] = nx(2 * p);
      SB();
      wait_vm<WIN - 1>(); R[2 * p + 1] = nx(2 * p + 1);
    }
    SB();
  }
#pragma unroll
  for (int e = 0; e < 8; ++e) acc[e] = mfma(a[e], P, acc[e]);
}
__device__ __forceinline__ void acc_zero(f32x4 (&acc)[8]) {
#pragma unroll
  for (int e = 0; e < 8; ++e) acc[e] = f32x4{0.f, 0.f, 0.f, 0.f};
}
__device__ __forceinline__ int k_block(int n, int w, int r, int par) {
  return n < 6 ? r * 128 + 4 * n + w : (n < 19 ? r * 128 - 48 : (r ^ 1) * 128 - 152) + 24 + 8 * n + 2 * w + par;
}

constexpr int L_FLAG = 0;
constexpr int L_T = 31;
constexpr int L_QA = 32;
constexpr int L_QB = L_QA + 128;
constexpr int L_OB = L_QB + 512;
constexpr int L_G = L_OB + 512;
constexpr int L_MLB = L_G + 16;
constexpr int L_RB = L_MLB + 64;
constexpr int L_RF = L_RB + 4 * 8 * RSTR;
constexpr int L_RS = L_RF + 4 * 128;
constexpr int L_RY = L_RS + 4 * 128;
constexpr int L_SC = L_RY + 4096;
constexpr int L_KT = L_SC + 4096;
constexpr int L_END = L_KT + 4 * 2048;
constexpr int L_WORDS = L_END;
static_assert(L_WORDS * 4 > 81920 && L_WORDS * 4 <= 160 * 1024, "lds");

typedef volatile __attribute__((address_space(3))) unsigned lflag_t;
__device__ __forceinline__ unsigned lds_wait(lflag_t* f) {
  unsigned v;
  for (unsigned spin = 0; (v = __builtin_amdgcn_readfirstlane(*f)) == 0u && spin < (SPIN_CAP << 4); ++spin) __builtin_amdgcn_s_sleep(2);
  asm volatile("" ::: "memory");
  return v;
}
__device__ __forceinline__ void lds_wait_gt(lflag_t* f, unsigned n) {
  for (unsigned spin = 0; (unsigned)__builtin_amdgcn_readfirstlane(*f) <= n && spin < (SPIN_CAP << 4); ++spin) __builtin_amdgcn_s_sleep(2);
  asm volatile("" ::: "memory");
}
__device__ __forceinline__ unsigned now() { return (unsigned)__builtin_amdgcn_s_memrealtime(); }
__device__ __forceinline__ void sleep_until(unsigned t) { while ((int)(now() - t) < 0) __builtin_amdgcn_s_sleep(2); }
__device__ __forceinline__ void lds_post(lflag_t* f, unsigned v = 1u) {
  asm volatile("s_waitcnt lgkmcnt(0)" ::: "memory");
  *f = v;
}
template <int NR> __device__ __forceinline__ void merge_recs(const float* ml, int mlstr, const float* rows, int rstr, float& M, float& L, f32x4& sum) {
  constexpr float LOG2E = 1.4426950408889634f;
  f32x2 r[NR];
#pragma unroll
  for (int k = 0; k < NR; ++k) r[k] = *(const f32x2*)(ml + k * mlstr);
  M = r[0].x;
#pragma unroll
  for (int k = 1; k < NR; ++k) M = fmaxf(M, r[k].x);
  sum = f32x4{0.f, 0.f, 0.f, 0.f}; L = 0.f;
#pragma unroll
  for (int k = 0; k < NR; ++k) {
    const float wt = __builtin_amdgcn_exp2f((r[k].x - M) * LOG2E);
    L = fmaf(wt, r[k].y, L);
    sum += wt * *(const f32x4*)(rows + k * rstr);
  }
}

__global__ void __launch_bounds__(512, 2) fused_decode(Params p) {
  __shared__ __attribute__((aligned(16))) float lds[L_WORDS];
  const int tid = threadIdx.x, lane = tid & 63, w = __builtin_amdgcn_readfirstlane(tid >> 6);
  const int g = lane >> 4, c = lane & 15, c7 = lane & 7, hi = c >> 3;
  const int b = blockIdx.x >> 5;
  const int h = (blockIdx.x & 26) | ((blockIdx.x & 1) << 2) | (((blockIdx.x >> 2) ^ (blockIdx.x >> HX)) & 1);
  const int me = b * 32 + h, role = h & 1;
  unsigned* const ldu = (unsigned*)lds;
  lflag_t* const flag = (lflag_t*)lds + L_FLAG;
  const rsrc_t rws = mk_rsrc(p.ws, 0, WS_END);
  const int rec_n0 = tile_n0((lane >> 3) & 3, (lane >> 1) & 3, lane & 1);
  STAMP(0);
  if (tid < 32) ldu[L_FLAG + tid] = 0u;
  wg_barrier();

  if (w >= 6) {
    const unsigned ep = __builtin_amdgcn_raw_buffer_load_b32(rws, WS_EPOCH, 0, AUX_SC1);
    const unsigned rnd = (unsigned)__builtin_amdgcn_s_memtime();
    auto probe = [&](const void* tensor, int k, int par) -> unsigned {
      const char* a = (const char*)tensor + ((size_t)me << 20) + (32 + ((rnd + 7 * k) & 31)) * 16384 + par * 8192 + (((rnd >> 5) + 13 * k) & 63) * 128;
      unsigned long long t0, t1; unsigned v;
      asm volatile("s_memtime %0\n\ts_waitcnt lgkmcnt(0)\n\ts_load_dword %2, %3, 0x0\n\ts_waitcnt lgkmcnt(0)\n\ts_memtime %1\n\ts_waitcnt lgkmcnt(0)"
                   : "=&s"(t0), "=&s"(t1), "=&s"(v) : "s"(a) : "memory");
      const unsigned dt = (unsigned)(t1 - t0);
      return dt < 520u ? 4000u : dt;
    };
    unsigned m0 = 4000u, m1 = 4000u;
    for (int k = 0; k < NPK; ++k) m0 = min(m0, probe(p.kA, k, w - 6));
    if (w == 7) {
      lds_post(flag + 20, m0);
      m0 = 4000u;
      for (int k = 0; k < NPV; ++k) { const unsigned dt = probe(p.vA, k, k & 1); if (k & 1) m1 = min(m1, dt); else m0 = min(m0, dt); }
    } else m1 = lds_wait(flag + 20);
    STAMP(1);
    const unsigned T = (unsigned)__builtin_amdgcn_readfirstlane(ep) + TAG_INC;
    const unsigned d = m0 + 4096u - m1;
    if (lane == 0) st_sc1(rws, WS_PR + ((me ^ 1) * 4 + (w - 6)) * 128, tag_raw<0xffu>(v4u{d << 8, 0u, 0u, 0u}, T));
    drain();
    sleep_until(now() + 40);
    v4u u[1];
    poll<0xffu, 1, 16>(rws, [&](int) { return WS_PR + (me * 4 + (w - 6)) * 128; }, u, T);
    const unsigned dp = (unsigned)__builtin_amdgcn_readfirstlane(u[0].x) >> 8;
    unsigned par = (role ? dp < d : d < dp) ? 0u : 1u;
    if (FORCE >= 0) par = (FORCE >> (w - 6)) & 1;
    par ^= role;
    lds_post(flag + (w - 4), 1u + par);
    STAMP(2);
    if (w == 6) {
      for (int k = 0; k < 6; ++k) __builtin_amdgcn_s_sleep(127);
      lds_wait(flag + 16); lds_wait(flag + 17); lds_wait(flag + 18); lds_wait(flag + 19);
      STAMP(3);
#pragma unroll
      for (int k = 0; k < 8; ++k)
        st_sc1(rws, WS_IN + (me ^ 1) * 8192 + k * 1024 + lane * 16, *(const v4u*)(ldu + L_SC + k_block(4 * k + g, c >> 2, role, par) * 16 + (c & 3) * 4));
      drain();
      if (lane == 0) st_sc1(rws, WS_PR + ((me ^ 1) * 4 + 2) * 128, tag_raw<0xffu>(v4u{0u, 0u, 0u, 0u}, T));
      drain();
      sleep_until(now() + 40);
      poll<0xffu, 1, 16>(rws, [&](int) { return WS_PR + (me * 4 + 2) * 128; }, u, T);
      __builtin_amdgcn_fence(__ATOMIC_ACQUIRE, "wavefront");
      v4u x[8];
#pragma unroll
      for (int k = 0; k < 8; ++k) x[k] = ld_sc1(rws, WS_IN + me * 8192 + k * 1024 + lane * 16);
#pragma unroll
      for (int k = 0; k < 8; ++k) *(v4u*)(ldu + L_SC + k_block(4 * k + g, c >> 2, role ^ 1, par ^ 1) * 16 + (c & 3) * 4) = x[k];
      lds_post(flag + 21);
      STAMP(4);
    } else {
      for (int k = 0; k < 6; ++k) __builtin_amdgcn_s_sleep(127);
      lds_wait(flag + 21);
    }
    {
      unsigned* tb = ldu + L_SC + ((role ^ (w & 1) ^ 1) * 128) * 16 + lane * 4;
      float e[64];
#pragma unroll
      for (int k = 0; k < 8; ++k) {
        const v4u x = *(const v4u*)(tb + k * 256);
#pragma unroll
        for (int i = 0; i < 4; ++i) { e[8 * k + 2 * i] = u2f(x[i] << 16); e[8 * k + 2 * i + 1] = u2f(x[i] & 0xffff0000u); }
      }
      float M = e[0];
#pragma unroll
      for (int i = 1; i < 64; ++i) M = fmaxf(M, e[i]);
      M = fmaxf(M, dpp<0x128>(M)); M = fmaxf(M, dpp<0x124>(M)); M = fmaxf(M, dpp<0x122>(M)); M = fmaxf(M, dpp<0x121>(M));
      M = xrow_max(M);
      float L = 0.f;
#pragma unroll
      for (int i = 0; i < 64; ++i) { e[i] = __builtin_amdgcn_exp2f((e[i] - M) * 1.4426950408889634f); L += e[i]; }
      L = 1.f / xrow_sum(row_allsum(L));
#pragma unroll
      for (int k = 0; k < 8; ++k)
        *(v4u*)(tb + k * 256) = v4u{pk_bf16(e[8 * k] * L, e[8 * k + 1] * L), pk_bf16(e[8 * k + 2] * L, e[8 * k + 3] * L),
                                    pk_bf16(e[8 * k + 4] * L, e[8 * k + 5] * L), pk_bf16(e[8 * k + 6] * L, e[8 * k + 7] * L)};
      lds_post(flag + 18 + w);
    }
    STAMP(5);
  } else if (w >= 4) {
    __builtin_amdgcn_s_setprio(3);
    const int mat = w - 4;
    const rsrc_t rx = mk_rsrc(p.x, 0, 8 * 1024 * 2);
    if (mat) __builtin_amdgcn_s_sleep(8);
    const unsigned ep = __builtin_amdgcn_raw_buffer_load_b32(rws, WS_EPOCH, 0, AUX_SC1);
    v4u xf[4], W[32];
#pragma unroll
    for (int kb = 0; kb < 4; ++kb) xf[kb] = ld_plain(rx, c7 * 2048 + (128 * b + 32 * kb + 8 * g) * 2, 0);
    {
      const rsrc_t rq = mk_rsrc(mat ? p.wqB : p.wqA, (size_t)(128 * b) * 8192 + 256 * h, 127 * 8192 + 256);
      const int vo = 8 * g * 8192 + 16 * c;
#pragma unroll
      for (int j = 0; j < 32; ++j) { W[j] = ld_nt(rq, vo, 8192 * (32 * (j >> 3) + (j & 7))); SB(); }
      PIN();
    }
    const unsigned T = (unsigned)__builtin_amdgcn_readfirstlane(ep) + TAG_INC;
    {
      f32x4 d[8];
#pragma unroll
      for (int e = 0; e < 8; ++e) d[e] = f32x4{0.f, 0.f, 0.f, 0.f};
#pragma unroll
      for (int kb = 0; kb < 4; ++kb) {
        v4u a[8];
#pragma unroll
        for (int q = 0; q < 4; ++q) tr_pair(W[8 * kb + 2 * q], W[8 * kb + 2 * q + 1], a, q);
#pragma unroll
        for (int e = 0; e < 8; ++e) d[e] = mfma(a[e], xf[kb], d[e]);
        SB();
      }
      STAMP(1);
      const int vo = WS_QPART + ((((mat * 32 + h) * 8 + c7) * 8 + b) * 128 + g * 8 + hi * 4) * 4;
#pragma unroll
      for (int k = 0; k < 4; ++k) st_sc1(rws, vo + 128 * k, tag_f32<3u>(tile_vec(d, k, hi), T));
    }
    STAMP(2);
    drain();
    auto gather_q = [&](int head) -> f32x4 {
      v4u u[4];
      const int base = WS_QPART + ((mat * 32 + head) * 8 + b) * 4096 + lane * 16;
      poll<3u, 4, 16>(rws, [&](int k) { return base + 1024 * k; }, u, T);
      f32x4 q = (untag<3u>(u[0]) + untag<3u>(u[1])) + (untag<3u>(u[2]) + untag<3u>(u[3]));
#pragma unroll
      for (int i = 0; i < 4; ++i) q[i] = half_sum(q[i]);
      return q;
    };
    sleep_until(now() + (mat ? 400 : QDELAY));
    if (mat == 0) {
      const f32x4 qs = gather_q(h);
      if (lane < 32) *(v2u*)(ldu + L_QA + rec_n0 / 2) = v2u{pk_bf16(qs[0], qs[1]), pk_bf16(qs[2], qs[3])};
      if (lane == 0) ldu[L_T] = T;
      lds_post(flag + 0, 1u);
      STAMP(3);
      __builtin_amdgcn_s_setprio(0);
      sleep_until(now() + 300);
      const f32x4 qf = gather_q(h ^ 1);
      if (lane < 32) *(v2u*)(ldu + L_QA + 64 + rec_n0 / 2) = v2u{pk_bf16(qf[0], qf[1]), pk_bf16(qf[2], qf[3])};
      lds_post(flag + 12, 1u);
      {
        const rsrc_t roa = mk_rsrc(p.woA, (size_t)(128 * h) * 2048 + 256 * b, 64);
        const rsrc_t rob = mk_rsrc(p.woB, (size_t)(128 * h) * 2048 + 256 * b, 64);
        const rsrc_t ro = mk_rsrc(p.out, 0, 64);
        unsigned t0 = __builtin_amdgcn_raw_buffer_load_b32(roa, 0, 0, AUX_NT);
        unsigned t1 = __builtin_amdgcn_raw_buffer_load_b32(rob, 0, 0, AUX_NT);
        unsigned t2 = __builtin_amdgcn_raw_buffer_load_b32(ro, 0, 0, AUX_NT);
        asm volatile("" :: "v"(t0), "v"(t1), "v"(t2));
      }
      for (int k = 0; k < 12; ++k) __builtin_amdgcn_s_sleep(127);
      lds_wait(flag + 8); lds_wait(flag + 9); lds_wait(flag + 10); lds_wait(flag + 11);
      STAMP(4);
      auto sum4 = [&](int rows) {
        const float* r = lds + rows + 4 * (lane & 31);
        return (*(const f32x4*)(r) + *(const f32x4*)(r + 128)) + (*(const f32x4*)(r + 256) + *(const f32x4*)(r + 384));
      };
      {
        const f32x4 sum = sum4(L_RF);
        const f32x4 hi4 = untag<0xffu>(__builtin_bit_cast(v4u, sum));
        st_sc1(rws, WS_AP + (me ^ 1) * 1280 + lane * 16, tag_raw<0xffu>(__builtin_bit_cast(v4u, lane < 32 ? hi4 : sum - hi4), T));
      }
      sleep_until(now() + 500);
      f32x4 rp;
      {
        v4u u[1];
        poll<0xffu, 1, 64>(rws, [&](int) { return WS_AP + me * 1280 + lane * 16; }, u, T);
        rp = untag<0xffu>(u[0]);
#pragma unroll
        for (int i = 0; i < 4; ++i) rp[i] = half_sum(rp[i]);
      }
      STAMP(5);
      lds_wait(flag + 26);
      __builtin_amdgcn_s_setprio(3);
      const int oo = lane < 32 ? WS_O + ((h * 8 + b) * 128 + rec_n0) * 4 : OOB;
      for (unsigned spin = 0; spin < (SPIN_CAP << 4); ++spin) {
        const unsigned f = flag[13] & flag[14] & flag[15] & flag[23];
        if (__builtin_amdgcn_readfirstlane(f)) break;
      }
      asm volatile("" ::: "memory");
      STAMP(6);
      st_sc1(rws, oo, tag_bf16(sum4(L_RS) + rp, T));
      STAMP(7);
    } else {
      {
        const f32x4 q = gather_q(h);
        if (lane < 32) st_sc1(rws, WS_QB + (me * 128 + rec_n0) * 4, tag_bf16(q, T));
      }
      {
        const rsrc_t rg = mk_rsrc(p.gate, 0, 2048);
        const v4u x0 = ld_plain(rx, b * 2048 + lane * 32, 0), x1 = ld_plain(rx, b * 2048 + lane * 32 + 16, 0);
        const v4u g0 = ld_plain(rg, lane * 32, 0), g1 = ld_plain(rg, lane * 32 + 16, 0);
        float gs = 0.f;
#pragma unroll
        for (int i = 0; i < 4; ++i) {
          gs += rnd_bf16(u2f(x0[i] << 16) * u2f(g0[i] << 16)) + rnd_bf16(u2f(x0[i] & 0xffff0000u) * u2f(g0[i] & 0xffff0000u));
          gs += rnd_bf16(u2f(x1[i] << 16) * u2f(g1[i] << 16)) + rnd_bf16(u2f(x1[i] & 0xffff0000u) * u2f(g1[i] & 0xffff0000u));
        }
        gs = rnd_bf16(xrow_sum(row_allsum(gs)));
        const float gt = rnd_bf16(1.f / (1.f + __expf(-gs)));
        if (lane == 0) st_sc1(rws, WS_G + (h * 8 + b) * 16, tag_bf16(f32x4{gt, 1.f - gt, 0.f, 0.f}, T));
      }
      __builtin_amdgcn_s_setprio(0);
      sleep_until(now() + 800);
      {
        v4u u[4];
        const int base = WS_QB + (b * 32 + (h & 24)) * 512 + lane * 16;
        poll<0xffu, 4, 16>(rws, [&](int k) { return base + 1024 * k; }, u, T);
#pragma unroll
        for (int k = 0; k < 4; ++k) *(v2u*)(ldu + L_QB + 128 * k + 2 * lane) = pack_hi(u[k]);
        lds_post(flag + 1);
      }
      STAMP(3);
      for (int k = 0; k < 6; ++k) __builtin_amdgcn_s_sleep(127);
      lds_wait(flag + 4); lds_wait(flag + 5); lds_wait(flag + 6); lds_wait(flag + 7);
      STAMP(4);
#pragma unroll
      for (int k = 0; k < 4; ++k) {
        const int hd = 2 * k + (lane >> 5), n4 = 4 * (lane & 31);
        float M, L; f32x4 sum;
        merge_recs<4>(lds + L_MLB + hd * 2, 16, lds + L_RB + hd * RSTR + n4, 8 * RSTR, M, L, sum);
        const int rec = (b * 32 + (h & 24) + hd) * 8 + (h & 7);
        st_sc1(rws, WS_BACC + (rec * 128 + n4) * 4, tag_f32<0xffu>(sum, T));
        if (n4 == 0) {
          const float Mh = u2f(f2u(M) & 0xffffff00u);
          const v4u r = {f2u(Mh), f2u(M - Mh), f2u(L) + 0x80u, 0u};
          st_sc1(rws, WS_BML + rec * 16, tag_raw<0xffu>(r, T));
        }
      }
      sleep_until(now() + 800);
      {
        v4u u[5];
        const int rec = me * 8;
        const int hf = lane >> 5;
        poll<0xffu, 5, 64>(rws, [&](int k) { return k < 4 ? WS_BACC + ((rec + 2 * k + hf) * 128 + 4 * (lane & 31)) * 4 : WS_BML + (rec + c7) * 16; }, u, T);
        const f32x4 r = untag<0xffu>(u[4]);
        const float ms = r.x + r.y;
        float M = fmaxf(ms, dpp<0x124>(ms)); M = fmaxf(M, dpp<0x122>(M)); M = fmaxf(M, dpp<0x121>(M));
        const float wt = __builtin_amdgcn_exp2f((ms - M) * 1.4426950408889634f);
        float L = wt * r.z; L += dpp<0x124>(L); L += dpp<0x122>(L); L += dpp<0x121>(L);
        f32x4 sum = {0.f, 0.f, 0.f, 0.f};
#pragma unroll
        for (int k = 0; k < 4; ++k) sum += u2f((unsigned)__builtin_amdgcn_ds_bpermute((2 * k + hf) * 4, (int)f2u(wt))) * untag<0xffu>(u[k]);
#pragma unroll
        for (int i = 0; i < 4; ++i) sum[i] = half_sum(sum[i]);
        if (lane < 32) st_sc1(rws, WS_O + (((32 + h) * 8 + b) * 128 + rec_n0) * 4, tag_bf16(sum * (1.f / L), T));
      }
      STAMP(5);
      sleep_until(now() + 800);
      {
        v4u u[5];
        const int base = WS_O + (32 + h) * 4096 + lane * 16;
        poll<0xffu, 5, 64>(rws, [&](int k) { return k < 4 ? base + 1024 * k : WS_G + (h * 8 + c7) * 16; }, u, T);
#pragma unroll
        for (int k = 0; k < 4; ++k) *(v2u*)(ldu + L_OB + 128 * k + 2 * lane) = pack_hi(u[k]);
        if (lane < 8) *(v2u*)(ldu + L_G + 2 * lane) = v2u{u[4].x & 0xffff0000u, u[4].y & 0xffff0000u};
        lds_post(flag + 22);
      }
      STAMP(6);
    }
  } else {
    Pacer pc;
    pc.t0 = (unsigned)__builtin_amdgcn_s_memrealtime();
    pc.acc = PT0; pc.inc = P1;
    const rsrc_t rkA = mk_rsrc(p.kA, (size_t)(me & ~1) << 20, 2 << 20);
    const rsrc_t rvA = mk_rsrc(p.vA, (size_t)(me & ~1) << 20, 2 << 20);
    const int lin = lane * 16;
    auto ldR = [&](rsrc_t r, int soff) { return [=](int j) { return ld_nt(r, lin + 1024 * j, soff); }; };
    unsigned* const sc = ldu + L_SC + g * 4;
    v4u X[8], Y[8];
    unsigned T;
    v4u q1, q2;
    int myK = 0;
    auto kblk = [&](int n) { return k_block(n, w, role, myK); };
    auto emit = [&](int n, v4u sv) { if (c == 0) *(v4u*)(sc + kblk(n) * 16) = sv; };
    {
      v4u X1[8], Y1[8];
      auto pre = [&](v4u (&R)[8], int n) {
#pragma unroll
        for (int j = 0; j < 8; ++j) { if ((j & 3) == 0) pc.tick(); wait_vm<WIN - 1>(); R[j] = ldR(rkA, kblk(n) * 8192)(j); SB(); }
      };
      pre(X, 0); pre(Y, 1); pre(X1, 2); pre(Y1, 3);
      PIN();
      STAMP(1);
      lds_wait(flag + 0);
      q1 = *(const v4u*)(ldu + L_QA + 4 * c);
      T = (unsigned)__builtin_amdgcn_readfirstlane(ldu[L_T]);
      STAMP(2);
      emit(0, k_part<true>(X, q1, pc, ldR(rkA, kblk(4) * 8192)));
      emit(2, k_part<false>(X1, q1, pc, ldR(rkA, 0)));
      emit(1, k_part<true>(Y, q1, pc, ldR(rkA, kblk(5) * 8192)));
      emit(3, k_part<false>(Y1, q1, pc, ldR(rkA, 0)));
    }
    STAMP(3);
    myK = lds_wait(flag + 2) - 1;
    pc.inc = P2;
    STAMP(4);
    for (int n = 4; n < 18; n += 2) {
      emit(n, k_part<true>(X, q1, pc, ldR(rkA, kblk(n + 2) * 8192)));
      emit(n + 1, k_part<true>(Y, q1, pc, ldR(rkA, kblk(n + 3) * 8192)));
    }
    lds_wait(flag + 12);
    q2 = *(const v4u*)(ldu + L_QA + 64 + 4 * c);
    for (int n = 18; n < 30; n += 2) {
      emit(n, k_part<true>(X, q1, pc, ldR(rkA, kblk(n + 2) * 8192)));
      emit(n + 1, k_part<true>(Y, q2, pc, ldR(rkA, kblk(n + 3) * 8192)));
      q1 = q2;
    }
    f32x4 acc[8];
    int vb;
    {
      const rsrc_t rkB = mk_rsrc(p.kB, (size_t)me << 17, 1 << 17);
      const rsrc_t rvB = mk_rsrc(p.vB, (size_t)me << 17, 1 << 17);
      const int wb = w * 8192;
      pc.inc = P1;
      STAMP(5);
      emit(30, k_part<true>(X, q2, pc, ldR(rkB, wb)));
      emit(31, k_part<true>(Y, q2, pc, ldR(rkB, wb + 32768)));
      lds_post(flag + 16 + w);
      lds_wait(flag + 1);
      v4u QB[4], PB[4];
#pragma unroll
      for (int k = 0; k < 4; ++k) QB[k] = *(const v4u*)(ldu + L_QB + 64 * c7 + 4 * (4 * k + g));
      unsigned* const tile = ldu + L_KT + w * 2048;
      PB[0] = k_part8(X, QB, tile, g, c, pc, ldR(rkB, wb + 2 * 32768));
      PB[1] = k_part8(Y, QB, tile, g, c, pc, ldR(rkB, wb + 3 * 32768));
      PB[2] = k_part8(X, QB, tile, g, c, pc, ldR(rvB, wb));
      PB[3] = k_part8(Y, QB, tile, g, c, pc, ldR(rvB, wb + 32768));
      float mB, lB = 0.f;
      {
        float e[32];
#pragma unroll
        for (int i = 0; i < 16; ++i) { e[2 * i] = u2f(PB[i >> 2][i & 3] << 16); e[2 * i + 1] = u2f(PB[i >> 2][i & 3] & 0xffff0000u); }
        mB = e[0];
#pragma unroll
        for (int i = 1; i < 32; ++i) mB = fmaxf(mB, e[i]);
        mB = xrow_max(mB);
#pragma unroll
        for (int i = 0; i < 32; ++i) { e[i] = __builtin_amdgcn_exp2f((e[i] - mB) * 1.4426950408889634f); lB += e[i]; }
#pragma unroll
        for (int i = 0; i < 16; ++i) PB[i >> 2][i & 3] = pk_bf16(e[2 * i], e[2 * i + 1]);
        lB = xrow_sum(lB);
        if (lane < 8) *(f32x2*)(lds + L_MLB + (w * 8 + lane) * 2) = f32x2{mB, lB};
      }
      acc_zero(acc);
      STAMP(6);
      v_part<true>(X, PB[0], acc, pc, ldR(rvB, wb + 2 * 32768));
      v_part<true>(Y, PB[1], acc, pc, ldR(rvB, wb + 3 * 32768));
      vb = (role ^ 1) * 128 + 2 * w + (lds_wait(flag + 3) - 1);
      pc.inc = P2;
      v_part<true>(X, PB[2], acc, pc, ldR(rvA, vb * 8192));
      v_part<true>(Y, PB[3], acc, pc, ldR(rvA, (vb + 8) * 8192));
#pragma unroll
      for (int k = 0; k < 4; ++k) *(f32x4*)(lds + L_RB + (w * 8 + c7) * RSTR + k * 32 + g * 8 + hi * 4) = tile_vec(acc, k, hi);
      lds_post(flag + 4 + w);
    }
    auto vblk = [&](int v) { return (vb + 8 * v) & 255; };
    auto prob = [&](int v) { return *(const v4u*)(sc + vblk(v) * 16); };
    auto dump_one = [&](int rows) {
      if (c7 == 0) {
#pragma unroll
        for (int k = 0; k < 4; ++k) *(f32x4*)(lds + rows + w * 128 + k * 32 + g * 8 + hi * 4) = tile_vec(acc, k, hi);
      }
    };
    acc_zero(acc);
    STAMP(7);
    lds_wait(flag + 24);
    STAMP(8);
    for (int v = 0; v < 16; v += 2) {
      v_part<true>(X, prob(v), acc, pc, ldR(rvA, vblk(v + 2) * 8192));
      v_part<true>(Y, prob(v + 1), acc, pc, ldR(rvA, vblk(v + 3) * 8192));
    }
    dump_one(L_RF);
    lds_post(flag + 8 + w);
    acc_zero(acc);
    lds_wait(flag + 25);
    for (int v = 16; v < 30; v += 2) {
      v_part<true>(X, prob(v), acc, pc, ldR(rvA, vblk(v + 2) * 8192));
      if (v == 28) flag[26] = 1u;
      v_part<true>(Y, prob(v + 1), acc, pc, ldR(rvA, vblk(v + 3) * 8192));
    }
    STAMP(10);
    const rsrc_t roa = mk_rsrc(p.woA, (size_t)(128 * h) * 2048 + 256 * b, 127 * 2048 + 256);
    const rsrc_t rob = mk_rsrc(p.woB, (size_t)(128 * h) * 2048 + 256 * b, 127 * 2048 + 256);
    const int wo = (32 * w + 8 * g) * 2048 + 16 * c;
    v4u K[8], V[8];
    {
      const v4u p30 = prob(30), p31 = prob(31);
      v_part<false>(X, p30, acc, pc, ldR(rvA, 0));
      PIN();
#pragma unroll
      for (int j = 0; j < 8; ++j) { K[j] = ld_nt(rob, wo, 2048 * j); SB(); }
      PIN();
      v_part<false>(Y, p31, acc, pc, ldR(rvA, 0));
    }
    STAMP(11);
    dump_one(L_RS);
    lds_post(flag + 13 + (w == 3 ? 10 : w));
    PIN();
#pragma unroll
    for (int j = 0; j < 8; ++j) { V[j] = ld_nt(roa, wo, 2048 * j); SB(); }
    PIN();
    STAMP(12);
    {
      f32x4 y[8];
      lds_wait(flag + 22);
      const f32x2 gt = *(const f32x2*)(lds + L_G + 2 * c7);
      f32x4 yv[4];
      {
        const v4u oB = *(const v4u*)(ldu + L_OB + 64 * c7 + 16 * w + 4 * g);
        v4u a[8];
#pragma unroll
        for (int q = 0; q < 4; ++q) tr_pair(K[2 * q], K[2 * q + 1], a, q);
#pragma unroll
        for (int e = 0; e < 8; ++e) y[e] = mfma(a[e], oB, f32x4{0.f, 0.f, 0.f, 0.f});
#pragma unroll
        for (int k = 0; k < 4; ++k) yv[k] = gt.y * tile_vec(y, k, hi);
      }
      v4u a[8];
#pragma unroll
      for (int q = 0; q < 4; ++q) tr_pair(V[2 * q], V[2 * q + 1], a, q);
      asm volatile("" : "+v"(a[0]), "+v"(a[1]), "+v"(a[2]), "+v"(a[3]), "+v"(a[4]), "+v"(a[5]), "+v"(a[6]), "+v"(a[7]), "+v"(yv[0]), "+v"(yv[1]), "+v"(yv[2]), "+v"(yv[3]) :: "memory");
      {
        v4u u[2];
        const int oa = WS_O + ((h * 8 + c7) * 128 + 32 * w + 8 * g) * 4;
        poll<0xffu, 2>(rws, [&](int k) { return oa + 16 * k; }, u, T);
        const v2u o0 = pack_hi(u[0]), o1 = pack_hi(u[1]);
        const v4u oA = {o0.x, o0.y, o1.x, o1.y};
#pragma unroll
        for (int e = 0; e < 8; ++e) y[e] = mfma(a[e], oA, f32x4{0.f, 0.f, 0.f, 0.f});
      }
      STAMP(13);
#pragma unroll
      for (int k = 0; k < 4; ++k) *(f32x4*)(lds + L_RY + ((w * 4 + k) * 64 + lane) * 4) = yv[k] + gt.x * tile_vec(y, k, hi);
      wg_barrier();
      f32x4 sum = *(const f32x4*)(lds + L_RY + (w * 64 + lane) * 4);
#pragma unroll
      for (int ww = 1; ww < 4; ++ww) sum += *(const f32x4*)(lds + L_RY + ((ww * 4 + w) * 64 + lane) * 4);
      st_sc1(rws, WS_Y + (((b * 32 + c7 * 4 + w) * 32 + h) * 32 + g * 8 + hi * 4) * 4, tag_f32<0xffu>(sum, T));
    }
    STAMP(14);
    if (w == 0) {
      drain();
      v4u u[4];
      const int yb = WS_Y + me * 4096 + lane * 16;
      poll<0xffu, 4>(rws, [&](int k) { return yb + 1024 * k; }, u, T);
      f32x4 sum = (untag<0xffu>(u[0]) + untag<0xffu>(u[1])) + (untag<0xffu>(u[2]) + untag<0xffu>(u[3]));
#pragma unroll
      for (int i = 0; i < 4; ++i) sum[i] = xrow_sum(sum[i] + dpp<0x128>(sum[i]));
      const rsrc_t ro = mk_rsrc(p.out, 0, 8 * 1024 * 2);
      if (lane < 8) {
        const v2u o = {pk_bf16(sum[0], sum[1]), pk_bf16(sum[2], sum[3])};
        __builtin_amdgcn_raw_buffer_store_b64(o, ro, ((h >> 2) * 1024 + 128 * b + tile_n0(h & 3, lane >> 1, lane & 1)) * 2, 0, 0);
      }
      if (blockIdx.x == 0 && lane == 0) __builtin_amdgcn_raw_buffer_store_b32(T, rws, WS_EPOCH, 0, AUX_SC1);
    }
    STAMP(15);
  }
}

extern "C" void kernel_launch(void* const* d_in, const int* in_sizes, int n_in, void* d_out, int out_size,
                              void* d_ws, size_t ws_size, hipStream_t stream) {
  Params p;
  p.x = d_in[0]; p.kA = d_in[1]; p.vA = d_in[2]; p.wqA = d_in[3]; p.woA = d_in[4];
  p.kB = d_in[6]; p.vB = d_in[7]; p.wqB = d_in[8]; p.woB = d_in[9]; p.gate = d_in[10];
  p.out = d_out; p.ws = d_ws;
  hipLaunchKernelGGL(fused_decode, dim3(256), dim3(512), 0, stream, p);
}
```

```cpp
#define DIAG 0
#include <hip/hip_runtime.h>
#include <stdint.h>

#define WIN 8
#define P1 14600
#define P2 14000
#define PT0 (260 * 256)
#define NPK 9
#define NPV 17
#define QDELAY 100
#define HX 3
#define FORCE -1

typedef unsigned v4u __attribute__((ext_vector_type(4)));
typedef unsigned v2u __attribute__((ext_vector_type(2)));
typedef float f32x4 __attribute__((ext_vector_type(4)));
typedef float f32x2 __attribute__((ext_vector_type(2)));
typedef __bf16 bf16x8 __attribute__((ext_vector_type(8)));
typedef __bf16 bf16x2 __attribute__((ext_vector_type(2)));
typedef __amdgpu_buffer_rsrc_t rsrc_t;

constexpr int AUX_NT = 2, AUX_SC1 = 16;
constexpr int OOB = 0x40000000;
constexpr int WS_EPOCH = 0;
constexpr int WS_QPART = 4096;
constexpr int WS_QB = WS_QPART + 2 * 32 * 8 * 8 * 128 * 4;
constexpr int WS_BACC = WS_QB + 8 * 32 * 128 * 4;
constexpr int WS_BML = WS_BACC + 8 * 32 * 8 * 128 * 4;
constexpr int WS_O = WS_BML + 8 * 32 * 8 * 4 * 4;
constexpr int WS_G = WS_O + 2 * 32 * 8 * 128 * 4;
constexpr int WS_Y = WS_G + 32 * 8 * 4 * 4;
constexpr int WS_AP = WS_Y + 8 * 32 * 32 * 32 * 4;
constexpr int WS_PR = WS_AP + 256 * 320 * 4;
constexpr int WS_IN = WS_PR + 256 * 4 * 32 * 4;
constexpr int WS_END = WS_IN + 256 * 8192;
constexpr int RSTR = 132;
constexpr unsigned SPIN_CAP = 1u << 20;
constexpr unsigned TAG_INC = 0x9E3579B1u;

#define STAMP(i) do { if ((i) == DIAG) dgt = (unsigned)__builtin_amdgcn_s_memrealtime(); } while (0)

struct Params {
  const void* x; const void* kA; const void* vA; const void* wqA; const void* woA;
  const void* kB; const void* vB; const void* wqB; const void* woB; const void* gate;
  void* out; void* ws;
};

__device__ __forceinline__ rsrc_t mk_rsrc(const void* p, size_t off, int bytes) {
  return __builtin_amdgcn_make_buffer_rsrc((void*)((const char*)p + off), 0, bytes, 0x00020000);
}
__device__ __forceinline__ v4u ld_nt(rsrc_t r, int voff, int soff) { return __builtin_amdgcn_raw_buffer_load_b128(r, voff, soff, AUX_NT); }
__device__ __forceinline__ v4u ld_plain(rsrc_t r, int voff, int soff) { return __builtin_amdgcn_raw_buffer_load_b128(r, voff, soff, 0); }
__device__ __forceinline__ v4u ld_sc1(rsrc_t r, int voff, int soff = 0) { return __builtin_amdgcn_raw_buffer_load_b128(r, voff, soff, AUX_SC1); }
__device__ __forceinline__ void st_sc1(rsrc_t r, int voff, v4u v, int soff = 0) { __builtin_amdgcn_raw_buffer_store_b128(v, r, voff, soff, AUX_SC1); }
template <int N> __device__ __forceinline__ void wait_vm() {
  __builtin_amdgcn_s_waitcnt((N & 15) | ((N >> 4) << 14) | (7 << 4) | (15 << 8));
}
__device__ __forceinline__ void drain() { asm volatile("s_waitcnt vmcnt(0)" ::: "memory"); }
__device__ __forceinline__ void wg_barrier() { asm volatile("s_waitcnt lgkmcnt(0)\n\ts_barrier" ::: "memory"); }
#define PIN() do { asm volatile("" ::: "memory"); __builtin_amdgcn_sched_barrier(0); } while (0)
#define SB() __builtin_amdgcn_sched_barrier(0)

__device__ __forceinline__ float u2f(unsigned u) { return __builtin_bit_cast(float, u); }
__device__ __forceinline__ unsigned f2u(float f) { return __builtin_bit_cast(unsigned, f); }
__device__ __forceinline__ unsigned pk_bf16(float lo, float hi) {
  f32x2 v = {lo, hi};
  return __builtin_bit_cast(unsigned, __builtin_convertvector(v, bf16x2));
}
__device__ __forceinline__ float rnd_bf16(float x) { return u2f(pk_bf16(x, x) & 0xffff0000u); }
__device__ __forceinline__ f32x4 mfma(v4u a, v4u b, f32x4 c) {
  return __builtin_amdgcn_mfma_f32_16x16x32_bf16(__builtin_bit_cast(bf16x8, a), __builtin_bit_cast(bf16x8, b), c, 0, 0, 0);
}
__device__ __forceinline__ float dot2(unsigned a, unsigned b, float c) {
  return __builtin_amdgcn_fdot2_f32_bf16(__builtin_bit_cast(bf16x2, a), __builtin_bit_cast(bf16x2, b), c, false);
}
template <int CTRL> __device__ __forceinline__ float dpp(float x) {
  return u2f((unsigned)__builtin_amdgcn_mov_dpp((int)f2u(x), CTRL, 0xf, 0xf, true));
}
__device__ __forceinline__ float row_allsum(float x) {
  x += dpp<0x128>(x); x += dpp<0x124>(x); x += dpp<0x122>(x); x += dpp<0x121>(x); return x;
}
__device__ __forceinline__ float xrow_max(float x) {
  auto s = __builtin_amdgcn_permlane16_swap(f2u(x), f2u(x), false, false);
  x = fmaxf(u2f(s[0]), u2f(s[1]));
  auto t = __builtin_amdgcn_permlane32_swap(f2u(x), f2u(x), false, false);
  return fmaxf(u2f(t[0]), u2f(t[1]));
}
__device__ __forceinline__ float half_sum(float x) {
  auto t = __builtin_amdgcn_permlane32_swap(f2u(x), f2u(x), false, false);
  return u2f(t[0]) + u2f(t[1]);
}
__device__ __forceinline__ float xrow_sum(float x) {
  auto s = __builtin_amdgcn_permlane16_swap(f2u(x), f2u(x), false, false);
  return half_sum(u2f(s[0]) + u2f(s[1]));
}
template <unsigned M> __device__ __forceinline__ v4u tag_raw(v4u v, unsigned T) {
  v4u r;
  r.x = (v.x & ~M) | (T & M);
  r.y = (v.y & ~M) | ((T >> 8) & M);
  r.z = (v.z & ~M) | ((T >> 16) & M);
  r.w = (v.w & ~M) | ((T >> 24) & M);
  return r;
}
template <unsigned M> __device__ __forceinline__ v4u tag_f32(f32x4 v, unsigned T) {
  constexpr unsigned H = (M + 1) / 2;
  v4u u = {f2u(v.x) + H, f2u(v.y) + H, f2u(v.z) + H, f2u(v.w) + H};
  return tag_raw<M>(u, T);
}
__device__ __forceinline__ v4u tag_bf16(f32x4 v, unsigned T) {
  const unsigned a = pk_bf16(v.x, v.y), b = pk_bf16(v.z, v.w);
  v4u u = {a << 16, a & 0xffff0000u, b << 16, b & 0xffff0000u};
  return tag_raw<0xffu>(u, T);
}
template <unsigned M> __device__ __forceinline__ bool tag_ok(v4u v, unsigned T) {
  const unsigned lo = __builtin_amdgcn_perm(v.y, v.x, 0x0c0c0400u), hi = __builtin_amdgcn_perm(v.w, v.z, 0x04000c0cu);
  return (((lo | hi) ^ T) & (M * 0x01010101u)) == 0u;
}
template <unsigned M> __device__ __forceinline__ f32x4 untag(v4u v) {
  f32x4 r = {u2f(v.x & ~M), u2f(v.y & ~M), u2f(v.z & ~M), u2f(v.w & ~M)};
  return r;
}
__device__ __forceinline__ v2u pack_hi(v4u a) {
  return v2u{__builtin_amdgcn_perm(a.y, a.x, 0x07060302u), __builtin_amdgcn_perm(a.w, a.z, 0x07060302u)};
}
template <unsigned M, int N, int SLP = 2, class F> __device__ __forceinline__ void poll(rsrc_t ws, F off, v4u (&v)[N], unsigned T) {
  for (unsigned spin = 0;; ++spin) {
    asm volatile("" ::: "memory");
#pragma unroll
    for (int k = 0; k < N; ++k) v[k] = ld_sc1(ws, off(k));
    bool ok = true;
#pragma unroll
    for (int k = 0; k < N; ++k) ok &= tag_ok<M>(v[k], T);
    if (__builtin_amdgcn_ballot_w64(!ok) == 0ull || spin > SPIN_CAP) break;
    __builtin_amdgcn_s_sleep(SLP);
  }
}
__device__ __forceinline__ void tr_pair(const v4u& r0, const v4u& r1, v4u (&a)[8], int p) {
#pragma unroll
  for (int d = 0; d < 4; ++d) {
    a[2 * d][p] = __builtin_amdgcn_perm(r1[d], r0[d], 0x05040100u);
    a[2 * d + 1][p] = __builtin_amdgcn_perm(r1[d], r0[d], 0x07060302u);
  }
}
__device__ __forceinline__ f32x4 tile_vec(const f32x4 (&acc)[8], int k, bool hi) {
  f32x4 r;
#pragma unroll
  for (int e = 0; e < 4; ++e) r[e] = hi ? acc[4 * (k & 1) + e][2 * (k >> 1) + 1] : acc[4 * (k & 1) + e][2 * (k >> 1)];
  return r;
}
__device__ __forceinline__ int tile_n0(int k, int g, int hi) { return 32 * g + 16 * (k >> 1) + 8 * hi + 4 * (k & 1); }

struct Pacer {
  unsigned t0, acc, inc;
  __device__ __forceinline__ void tick() {
    while ((unsigned)__builtin_amdgcn_s_memrealtime() - t0 < (acc >> 8)) __builtin_amdgcn_s_sleep(1);
    acc += inc;
  }
};
__device__ __forceinline__ unsigned round_scores(float a, float b) {
  const unsigned u = pk_bf16(a, b);
  return pk_bf16(u2f(u << 16) * 0.08838834764831845f, u2f(u & 0xffff0000u) * 0.08838834764831845f);
}
template <bool RF, class F>
__device__ __forceinline__ v4u k_part(v4u (&R)[8], const v4u& q, Pacer& pc, F nx) {
  float s[8];
#pragma unroll
  for (int j = 0; j < 8; ++j) {
    float d = dot2(R[j].x, q.x, 0.f);
    d = dot2(R[j].y, q.y, d); d = dot2(R[j].z, q.z, d); d = dot2(R[j].w, q.w, d);
    asm volatile("" : "+v"(d) :: "memory");
    s[j] = d;
    SB();
    if constexpr (RF) { if ((j & 3) == 0) pc.tick(); wait_vm<WIN - 1>(); R[j] = nx(j); }
    SB();
  }
#pragma unroll
  for (int j = 0; j < 8; ++j) s[j] = row_allsum(s[j]);
  return v4u{round_scores(s[0], s[1]), round_scores(s[2], s[3]), round_scores(s[4], s[5]), round_scores(s[6], s[7])};
}
template <class F>
__device__ __forceinline__ v4u k_part8(v4u (&R)[8], const v4u (&Q)[4], unsigned* tile, int g, int c, Pacer& pc, F nx) {
#pragma unroll
  for (int j = 0; j < 8; ++j) {
    *(v4u*)(tile + (4 * j + g) * 64 + (c ^ (4 * g + (j & 3))) * 4) = R[j];
    PIN();
    if ((j & 3) == 0) pc.tick();
    wait_vm<WIN - 1>(); R[j] = nx(j);
    SB();
  }
  f32x4 d[2];
#pragma unroll
  for (int t = 0; t < 2; ++t) {
    d[t] = f32x4{0.f, 0.f, 0.f, 0.f};
#pragma unroll
    for (int k = 0; k < 4; ++k) d[t] = mfma(*(const v4u*)(tile + (16 * t + 4 * (c & 3) + (c >> 2)) * 64 + ((4 * k + g) ^ c) * 4), Q[k], d[t]);
  }
  v4u r = {round_scores(d[0][0], d[0][1]), round_scores(d[0][2], d[0][3]), round_scores(d[1][0], d[1][1]), round_scores(d[1][2], d[1][3])};
  asm volatile("" : "+v"(r) :: "memory");
  return r;
}
template <bool RF, class F>
__device__ __forceinline__ void v_part(v4u (&R)[8], const v4u& P, f32x4 (&acc)[8], Pacer& pc, F nx) {
  v4u a[8];
#pragma unroll
  for (int p = 0; p < 4; ++p) {
    tr_pair(R[2 * p], R[2 * p + 1], a, p);
    asm volatile("" : "+v"(a[0][p]), "+v"(a[1][p]), "+v"(a[2][p]), "+v"(a[3][p]), "+v"(a[4][p]), "+v"(a[5][p]), "+v"(a[6][p]), "+v"(a[7][p]) :: "memory");
    SB();
    if constexpr (RF) {
      if ((p & 1) == 0) pc.tick();
      wait_vm<WIN - 1>(); R[2 * p] = nx(2 * p);
      SB();
      wait_vm<WIN - 1>(); R[2 * p + 1] = nx(2 * p + 1);
    }
    SB();
  }
#pragma unroll
  for (int e = 0; e < 8; ++e) acc[e] = mfma(a[e], P, acc[e]);
}
__device__ __forceinline__ void acc_zero(f32x4 (&acc)[8]) {
#pragma unroll
  for (int e = 0; e < 8; ++e) acc[e] = f32x4{0.f, 0.f, 0.f, 0.f};
}
__device__ __forceinline__ int k_block(int n, int w, int r, int par) {
  return n < 6 ? r * 128 + 4 * n + w : (n < 19 ? r * 128 - 48 : (r ^ 1) * 128 - 152) + 24 + 8 * n + 2 * w + par;
}

constexpr int L_FLAG = 0;
constexpr int L_T = 31;
constexpr int L_QA = 32;
constexpr int L_QB = L_QA + 128;
constexpr int L_OB = L_QB + 512;
constexpr int L_G = L_OB + 512;
constexpr int L_MLB = L_G + 16;
constexpr int L_RB = L_MLB + 64;
constexpr int L_RF = L_RB + 4 * 8 * RSTR;
constexpr int L_RS = L_RF + 4 * 128;
constexpr int L_RY = L_RS + 4 * 128;
constexpr int L_SC = L_RY + 4096;
constexpr int L_KT = L_SC + 4096;
constexpr int L_END = L_KT + 4 * 2048;
constexpr int L_WORDS = L_END;
static_assert(L_WORDS * 4 > 81920 && L_WORDS * 4 <= 160 * 1024, "lds");

typedef volatile __attribute__((address_space(3))) unsigned lflag_t;
__device__ __forceinline__ unsigned lds_wait(lflag_t* f) {
  unsigned v;
  for (unsigned spin = 0; (v = __builtin_amdgcn_readfirstlane(*f)) == 0u && spin < (SPIN_CAP << 4); ++spin) __builtin_amdgcn_s_sleep(2);
  asm volatile("" ::: "memory");
  return v;
}
__device__ __forceinline__ void lds_wait_gt(lflag_t* f, unsigned n) {
  for (unsigned spin = 0; (unsigned)__builtin_amdgcn_readfirstlane(*f) <= n && spin < (SPIN_CAP << 4); ++spin) __builtin_amdgcn_s_sleep(2);
  asm volatile("" ::: "memory");
}
__device__ __forceinline__ unsigned now() { return (unsigned)__builtin_amdgcn_s_memrealtime(); }
__device__ __forceinline__ void sleep_until(unsigned t) { while ((int)(now() - t) < 0) __builtin_amdgcn_s_sleep(2); }
__device__ __forceinline__ void lds_post(lflag_t* f, unsigned v = 1u) {
  asm volatile("s_waitcnt lgkmcnt(0)" ::: "memory");
  *f = v;
}
template <int NR> __device__ __forceinline__ void merge_recs(const float* ml, int mlstr, const float* rows, int rstr, float& M, float& L, f32x4& sum) {
  constexpr float LOG2E = 1.4426950408889634f;
  f32x2 r[NR];
#pragma unroll
  for (int k = 0; k < NR; ++k) r[k] = *(const f32x2*)(ml + k * mlstr);
  M = r[0].x;
#pragma unroll
  for (int k = 1; k < NR; ++k) M = fmaxf(M, r[k].x);
  sum = f32x4{0.f, 0.f, 0.f, 0.f}; L = 0.f;
#pragma unroll
  for (int k = 0; k < NR; ++k) {
    const float wt = __builtin_amdgcn_exp2f((r[k].x - M) * LOG2E);
    L = fmaf(wt, r[k].y, L);
    sum += wt * *(const f32x4*)(rows + k * rstr);
  }
}

__global__ void __launch_bounds__(512, 2) fused_decode(Params p) {
  __shared__ __attribute__((aligned(16))) float lds[L_WORDS];
  const int tid = threadIdx.x, lane = tid & 63, w = __builtin_amdgcn_readfirstlane(tid >> 6);
  const int g = lane >> 4, c = lane & 15, c7 = lane & 7, hi = c >> 3;
  const int b = blockIdx.x >> 5;
  const int h = (blockIdx.x & 26) | ((blockIdx.x & 1) << 2) | (((blockIdx.x >> 2) ^ (blockIdx.x >> HX)) & 1);
  const int me = b * 32 + h, role = h & 1;
  unsigned* const ldu = (unsigned*)lds;
  lflag_t* const flag = (lflag_t*)lds + L_FLAG;
  const rsrc_t rws = mk_rsrc(p.ws, 0, WS_END);
  const int rec_n0 = tile_n0((lane >> 3) & 3, (lane >> 1) & 3, lane & 1);
  unsigned dgt = 0; const unsigned dg0 = (unsigned)__builtin_amdgcn_s_memrealtime();
  STAMP(0);
  if (tid < 32) ldu[L_FLAG + tid] = 0u;
  wg_barrier();

  if (w >= 6) {
    const unsigned ep = __builtin_amdgcn_raw_buffer_load_b32(rws, WS_EPOCH, 0, AUX_SC1);
    const unsigned rnd = (unsigned)__builtin_amdgcn_s_memtime();
    auto probe = [&](const void* tensor, int k, int par) -> unsigned {
      const char* a = (const char*)tensor + ((size_t)me << 20) + (32 + ((rnd + 7 * k) & 31)) * 16384 + par * 8192 + (((rnd >> 5) + 13 * k) & 63) * 128;
      unsigned long long t0, t1; unsigned v;
      asm volatile("s_memtime %0\n\ts_waitcnt lgkmcnt(0)\n\ts_load_dword %2, %3, 0x0\n\ts_waitcnt lgkmcnt(0)\n\ts_memtime %1\n\ts_waitcnt lgkmcnt(0)"
                   : "=&s"(t0), "=&s"(t1), "=&s"(v) : "s"(a) : "memory");
      const unsigned dt = (unsigned)(t1 - t0);
      return dt < 520u ? 4000u : dt;
    };
    unsigned m0 = 4000u, m1 = 4000u;
    for (int k = 0; k < NPK; ++k) m0 = min(m0, probe(p.kA, k, w - 6));
    if (w == 7) {
      lds_post(flag + 20, m0);
      m0 = 4000u;
      for (int k = 0; k < NPV; ++k) { const unsigned dt = probe(p.vA, k, k & 1); if (k & 1) m1 = min(m1, dt); else m0 = min(m0, dt); }
    } else m1 = lds_wait(flag + 20);
    STAMP(1);
    const unsigned T = (unsigned)__builtin_amdgcn_readfirstlane(ep) + TAG_INC;
    const unsigned d = m0 + 4096u - m1;
    if (lane == 0) st_sc1(rws, WS_PR + ((me ^ 1) * 4 + (w - 6)) * 128, tag_raw<0xffu>(v4u{d << 8, 0u, 0u, 0u}, T));
    drain();
    sleep_until(now() + 40);
    v4u u[1];
    poll<0xffu, 1, 16>(rws, [&](int) { return WS_PR + (me * 4 + (w - 6)) * 128; }, u, T);
    const unsigned dp = (unsigned)__builtin_amdgcn_readfirstlane(u[0].x) >> 8;
    unsigned par = (role ? dp < d : d < dp) ? 0u : 1u;
    if (FORCE >= 0) par = (FORCE >> (w - 6)) & 1;
    par ^= role;
    lds_post(flag + (w - 4), 1u + par);
    STAMP(2);
    if (w == 6) {
      for (int k = 0; k < 6; ++k) __builtin_amdgcn_s_sleep(127);
      lds_wait(flag + 16); lds_wait(flag + 17); lds_wait(flag + 18); lds_wait(flag + 19);
      STAMP(3);
#pragma unroll
      for (int k = 0; k < 8; ++k)
        st_sc1(rws, WS_IN + (me ^ 1) * 8192 + k * 1024 + lane * 16, *(const v4u*)(ldu + L_SC + k_block(4 * k + g, c >> 2, role, par) * 16 + (c & 3) * 4));
      drain();
      if (lane == 0) st_sc1(rws, WS_PR + ((me ^ 1) * 4 + 2) * 128, tag_raw<0xffu>(v4u{0u, 0u, 0u, 0u}, T));
      drain();
      sleep_until(now() + 40);
      poll<0xffu, 1, 16>(rws, [&](int) { return WS_PR + (me * 4 + 2) * 128; }, u, T);
      __builtin_amdgcn_fence(__ATOMIC_ACQUIRE, "wavefront");
      v4u x[8];
#pragma unroll
      for (int k = 0; k < 8; ++k) x[k] = ld_sc1(rws, WS_IN + me * 8192 + k * 1024 + lane * 16);
#pragma unroll
      for (int k = 0; k < 8; ++k) *(v4u*)(ldu + L_SC + k_block(4 * k + g, c >> 2, role ^ 1, par ^ 1) * 16 + (c & 3) * 4) = x[k];
      lds_post(flag + 21);
      STAMP(4);
    } else {
      for (int k = 0; k < 6; ++k) __builtin_amdgcn_s_sleep(127);
      lds_wait(flag + 21);
    }
    {
      unsigned* tb = ldu + L_SC + ((role ^ (w & 1) ^ 1) * 128) * 16 + lane * 4;
      float e[64];
#pragma unroll
      for (int k = 0; k < 8; ++k) {
        const v4u x = *(const v4u*)(tb + k * 256);
#pragma unroll
        for (int i = 0; i < 4; ++i) { e[8 * k + 2 * i] = u2f(x[i] << 16); e[8 * k + 2 * i + 1] = u2f(x[i] & 0xffff0000u); }
      }
      float M = e[0];
#pragma unroll
      for (int i = 1; i < 64; ++i) M = fmaxf(M, e[i]);
      M = fmaxf(M, dpp<0x128>(M)); M = fmaxf(M, dpp<0x124>(M)); M = fmaxf(M, dpp<0x122>(M)); M = fmaxf(M, dpp<0x121>(M));
      M = xrow_max(M);
      float L = 0.f;
#pragma unroll
      for (int i = 0; i < 64; ++i) { e[i] = __builtin_amdgcn_exp2f((e[i] - M) * 1.4426950408889634f); L += e[i]; }
      L = 1.f / xrow_sum(row_allsum(L));
#pragma unroll
      for (int k = 0; k < 8; ++k)
        *(v4u*)(tb + k * 256) = v4u{pk_bf16(e[8 * k] * L, e[8 * k + 1] * L), pk_bf16(e[8 * k + 2] * L, e[8 * k + 3] * L),
                                    pk_bf16(e[8 * k + 4] * L, e[8 * k + 5] * L), pk_bf16(e[8 * k + 6] * L, e[8 * k + 7] * L)};
      lds_post(flag + 18 + w);
    }
    STAMP(5);
  } else if (w >= 4) {
    __builtin_amdgcn_s_setprio(3);
    const int mat = w - 4;
    const rsrc_t rx = mk_rsrc(p.x, 0, 8 * 1024 * 2);
    if (mat) __builtin_amdgcn_s_sleep(8);
    const unsigned ep = __builtin_amdgcn_raw_buffer_load_b32(rws, WS_EPOCH, 0, AUX_SC1);
    v4u xf[4], W[32];
#pragma unroll
    for (int kb = 0; kb < 4; ++kb) xf[kb] = ld_plain(rx, c7 * 2048 + (128 * b + 32 * kb + 8 * g) * 2, 0);
    {
      const rsrc_t rq = mk_rsrc(mat ? p.wqB : p.wqA, (size_t)(128 * b) * 8192 + 256 * h, 127 * 8192 + 256);
      const int vo = 8 * g * 8192 + 16 * c;
#pragma unroll
      for (int j = 0; j < 32; ++j) { W[j] = ld_nt(rq, vo, 8192 * (32 * (j >> 3) + (j & 7))); SB(); }
      PIN();
    }
    const unsigned T = (unsigned)__builtin_amdgcn_readfirstlane(ep) + TAG_INC;
    {
      f32x4 d[8];
#pragma unroll
      for (int e = 0; e < 8; ++e) d[e] = f32x4{0.f, 0.f, 0.f, 0.f};
#pragma unroll
      for (int kb = 0; kb < 4; ++kb) {
        v4u a[8];
#pragma unroll
        for (int q = 0; q < 4; ++q) tr_pair(W[8 * kb + 2 * q], W[8 * kb + 2 * q + 1], a, q);
#pragma unroll
        for (int e = 0; e < 8; ++e) d[e] = mfma(a[e], xf[kb], d[e]);
        SB();
      }
      STAMP(1);
      const int vo = WS_QPART + ((((mat * 32 + h) * 8 + c7) * 8 + b) * 128 + g * 8 + hi * 4) * 4;
#pragma unroll
      for (int k = 0; k < 4; ++k) st_sc1(rws, vo + 128 * k, tag_f32<3u>(tile_vec(d, k, hi), T));
    }
    STAMP(2);
    drain();
    auto gather_q = [&](int head) -> f32x4 {
      v4u u[4];
      const int base = WS_QPART + ((mat * 32 + head) * 8 + b) * 4096 + lane * 16;
      poll<3u, 4, 16>(rws, [&](int k) { return base + 1024 * k; }, u, T);
      f32x4 q = (untag<3u>(u[0]) + untag<3u>(u[1])) + (untag<3u>(u[2]) + untag<3u>(u[3]));
#pragma unroll
      for (int i = 0; i < 4; ++i) q[i] = half_sum(q[i]);
      return q;
    };
    sleep_until(now() + (mat ? 400 : QDELAY));
    if (mat == 0) {
      const f32x4 qs = gather_q(h);
      if (lane < 32) *(v2u*)(ldu + L_QA + rec_n0 / 2) = v2u{pk_bf16(qs[0], qs[1]), pk_bf16(qs[2], qs[3])};
      if (lane == 0) ldu[L_T] = T;
      lds_post(flag + 0, 1u);
      STAMP(3);
      __builtin_amdgcn_s_setprio(0);
      sleep_until(now() + 300);
      const f32x4 qf = gather_q(h ^ 1);
      if (lane < 32) *(v2u*)(ldu + L_QA + 64 + rec_n0 / 2) = v2u{pk_bf16(qf[0], qf[1]), pk_bf16(qf[2], qf[3])};
      lds_post(flag + 12, 1u);
      {
        const rsrc_t roa = mk_rsrc(p.woA, (size_t)(128 * h) * 2048 + 256 * b, 64);
        const rsrc_t rob = mk_rsrc(p.woB, (size_t)(128 * h) * 2048 + 256 * b, 64);
        const rsrc_t ro = mk_rsrc(p.out, 0, 64);
        unsigned t0 = __builtin_amdgcn_raw_buffer_load_b32(roa, 0, 0, AUX_NT);
        unsigned t1 = __builtin_amdgcn_raw_buffer_load_b32(rob, 0, 0, AUX_NT);
        unsigned t2 = __builtin_amdgcn_raw_buffer_load_b32(ro, 0, 0, AUX_NT);
        asm volatile("" :: "v"(t0), "v"(t1), "v"(t2));
      }
      for (int k = 0; k < 12; ++k) __builtin_amdgcn_s_sleep(127);
      lds_wait(flag + 8); lds_wait(flag + 9); lds_wait(flag + 10); lds_wait(flag + 11);
      STAMP(4);
      auto sum4 = [&](int rows) {
        const float* r = lds + rows + 4 * (lane & 31);
        return (*(const f32x4*)(r) + *(const f32x4*)(r + 128)) + (*(const f32x4*)(r + 256) + *(const f32x4*)(r + 384));
      };
      {
        const f32x4 sum = sum4(L_RF);
        const f32x4 hi4 = untag<0xffu>(__builtin_bit_cast(v4u, sum));
        st_sc1(rws, WS_AP + (me ^ 1) * 1280 + lane * 16, tag_raw<0xffu>(__builtin_bit_cast(v4u, lane < 32 ? hi4 : sum - hi4), T));
      }
      sleep_until(now() + 500);
      f32x4 rp;
      {
        v4u u[1];
        poll<0xffu, 1, 64>(rws, [&](int) { return WS_AP + me * 1280 + lane * 16; }, u, T);
        rp = untag<0xffu>(u[0]);
#pragma unroll
        for (int i = 0; i < 4; ++i) rp[i] = half_sum(rp[i]);
      }
      STAMP(5);
      lds_wait(flag + 26);
      __builtin_amdgcn_s_setprio(3);
      const int oo = lane < 32 ? WS_O + ((h * 8 + b) * 128 + rec_n0) * 4 : OOB;
      for (unsigned spin = 0; spin < (SPIN_CAP << 4); ++spin) {
        const unsigned f = flag[13] & flag[14] & flag[15] & flag[23];
        if (__builtin_amdgcn_readfirstlane(f)) break;
      }
      asm volatile("" ::: "memory");
      STAMP(6);
      st_sc1(rws, oo, tag_bf16(sum4(L_RS) + rp, T));
      STAMP(7);
    } else {
      {
        const f32x4 q = gather_q(h);
        if (lane < 32) st_sc1(rws, WS_QB + (me * 128 + rec_n0) * 4, tag_bf16(q, T));
      }
      {
        const rsrc_t rg = mk_rsrc(p.gate, 0, 2048);
        const v4u x0 = ld_plain(rx, b * 2048 + lane * 32, 0), x1 = ld_plain(rx, b * 2048 + lane * 32 + 16, 0);
        const v4u g0 = ld_plain(rg, lane * 32, 0), g1 = ld_plain(rg, lane * 32 + 16, 0);
        float gs = 0.f;
#pragma unroll
        for (int i = 0; i < 4; ++i) {
          gs += rnd_bf16(u2f(x0[i] << 16) * u2f(g0[i] << 16)) + rnd_bf16(u2f(x0[i] & 0xffff0000u) * u2f(g0[i] & 0xffff0000u));
          gs += rnd_bf16(u2f(x1[i] << 16) * u2f(g1[i] << 16)) + rnd_bf16(u2f(x1[i] & 0xffff0000u) * u2f(g1[i] & 0xffff0000u));
        }
        gs = rnd_bf16(xrow_sum(row_allsum(gs)));
        const float gt = rnd_bf16(1.f / (1.f + __expf(-gs)));
        if (lane == 0) st_sc1(rws, WS_G + (h * 8 + b) * 16, tag_bf16(f32x4{gt, 1.f - gt, 0.f, 0.f}, T));
      }
      __builtin_amdgcn_s_setprio(0);
      sleep_until(now() + 800);
      {
        v4u u[4];
        const int base = WS_QB + (b * 32 + (h & 24)) * 512 + lane * 16;
        poll<0xffu, 4, 16>(rws, [&](int k) { return base + 1024 * k; }, u, T);
#pragma unroll
        for (int k = 0; k < 4; ++k) *(v2u*)(ldu + L_QB + 128 * k + 2 * lane) = pack_hi(u[k]);
        lds_post(flag + 1);
      }
      STAMP(3);
      for (int k = 0; k < 6; ++k) __builtin_amdgcn_s_sleep(127);
      lds_wait(flag + 4); lds_wait(flag + 5); lds_wait(flag + 6); lds_wait(flag + 7);
      STAMP(4);
#pragma unroll
      for (int k = 0; k < 4; ++k) {
        const int hd = 2 * k + (lane >> 5), n4 = 4 * (lane & 31);
        float M, L; f32x4 sum;
        merge_recs<4>(lds + L_MLB + hd * 2, 16, lds + L_RB + hd * RSTR + n4, 8 * RSTR, M, L, sum);
        const int rec = (b * 32 + (h & 24) + hd) * 8 + (h & 7);
        st_sc1(rws, WS_BACC + (rec * 128 + n4) * 4, tag_f32<0xffu>(sum, T));
        if (n4 == 0) {
          const float Mh = u2f(f2u(M) & 0xffffff00u);
          const v4u r = {f2u(Mh), f2u(M - Mh), f2u(L) + 0x80u, 0u};
          st_sc1(rws, WS_BML + rec * 16, tag_raw<0xffu>(r, T));
        }
      }
      sleep_until(now() + 800);
      {
        v4u u[5];
        const int rec = me * 8;
        const int hf = lane >> 5;
        poll<0xffu, 5, 64>(rws, [&](int k) { return k < 4 ? WS_BACC + ((rec + 2 * k + hf) * 128 + 4 * (lane & 31)) * 4 : WS_BML + (rec + c7) * 16; }, u, T);
        const f32x4 r = untag<0xffu>(u[4]);
        const float ms = r.x + r.y;
        float M = fmaxf(ms, dpp<0x124>(ms)); M = fmaxf(M, dpp<0x122>(M)); M = fmaxf(M, dpp<0x121>(M));
        const float wt = __builtin_amdgcn_exp2f((ms - M) * 1.4426950408889634f);
        float L = wt * r.z; L += dpp<0x124>(L); L += dpp<0x122>(L); L += dpp<0x121>(L);
        f32x4 sum = {0.f, 0.f, 0.f, 0.f};
#pragma unroll
        for (int k = 0; k < 4; ++k) sum += u2f((unsigned)__builtin_amdgcn_ds_bpermute((2 * k + hf) * 4, (int)f2u(wt))) * untag<0xffu>(u[k]);
#pragma unroll
        for (int i = 0; i < 4; ++i) sum[i] = half_sum(sum[i]);
        if (lane < 32) st_sc1(rws, WS_O + (((32 + h) * 8 + b) * 128 + rec_n0) * 4, tag_bf16(sum * (1.f / L), T));
      }
      STAMP(5);
      sleep_until(now() + 800);
      {
        v4u u[5];
        const int base = WS_O + (32 + h) * 4096 + lane * 16;
        poll<0xffu, 5, 64>(rws, [&](int k) { return k < 4 ? base + 1024 * k : WS_G + (h * 8 + c7) * 16; }, u, T);
#pragma unroll
        for (int k = 0; k < 4; ++k) *(v2u*)(ldu + L_OB + 128 * k + 2 * lane) = pack_hi(u[k]);
        if (lane < 8) *(v2u*)(ldu + L_G + 2 * lane) = v2u{u[4].x & 0xffff0000u, u[4].y & 0xffff0000u};
        lds_post(flag + 22);
      }
      STAMP(6);
    }
  } else {
    Pacer pc;
    pc.t0 = (unsigned)__builtin_amdgcn_s_memrealtime();
    pc.acc = PT0; pc.inc = P1;
    const rsrc_t rkA = mk_rsrc(p.kA, (size_t)(me & ~1) << 20, 2 << 20);
    const rsrc_t rvA = mk_rsrc(p.vA, (size_t)(me & ~1) << 20, 2 << 20);
    const int lin = lane * 16;
    auto ldR = [&](rsrc_t r, int soff) { return [=](int j) { return ld_nt(r, lin + 1024 * j, soff); }; };
    unsigned* const sc = ldu + L_SC + g * 4;
    v4u X[8], Y[8];
    unsigned T;
    v4u q1, q2;
    int myK = 0;
    auto kblk = [&](int n) { return k_block(n, w, role, myK); };
    auto emit = [&](int n, v4u sv) { if (c == 0) *(v4u*)(sc + kblk(n) * 16) = sv; };
    {
      v4u X1[8], Y1[8];
      auto pre = [&](v4u (&R)[8], int n) {
#pragma unroll
        for (int j = 0; j < 8; ++j) { if ((j & 3) == 0) pc.tick(); wait_vm<WIN - 1>(); R[j] = ldR(rkA, kblk(n) * 8192)(j); SB(); }
      };
      pre(X, 0); pre(Y, 1); pre(X1, 2); pre(Y1, 3);
      PIN();
      STAMP(1);
      lds_wait(flag + 0);
      q1 = *(const v4u*)(ldu + L_QA + 4 * c);
      T = (unsigned)__builtin_amdgcn_readfirstlane(ldu[L_T]);
      STAMP(2);
      emit(0, k_part<true>(X, q1, pc, ldR(rkA, kblk(4) * 8192)));
      emit(2, k_part<false>(X1, q1, pc, ldR(rkA, 0)));
      emit(1, k_part<true>(Y, q1, pc, ldR(rkA, kblk(5) * 8192)));
      emit(3, k_part<false>(Y1, q1, pc, ldR(rkA, 0)));
    }
    STAMP(3);
    myK = lds_wait(flag + 2) - 1;
    pc.inc = P2;
    STAMP(4);
    for (int n = 4; n < 18; n += 2) {
      emit(n, k_part<true>(X, q1, pc, ldR(rkA, kblk(n + 2) * 8192)));
      emit(n + 1, k_part<true>(Y, q1, pc, ldR(rkA, kblk(n + 3) * 8192)));
    }
    lds_wait(flag + 12);
    q2 = *(const v4u*)(ldu + L_QA + 64 + 4 * c);
    for (int n = 18; n < 30; n += 2) {
      emit(n, k_part<true>(X, q1, pc, ldR(rkA, kblk(n + 2) * 8192)));
      emit(n + 1, k_part<true>(Y, q2, pc, ldR(rkA, kblk(n + 3) * 8192)));
      q1 = q2;
    }
    f32x4 acc[8];
    int vb;
    {
      const rsrc_t rkB = mk_rsrc(p.kB, (size_t)me << 17, 1 << 17);
      const rsrc_t rvB = mk_rsrc(p.vB, (size_t)me << 17, 1 << 17);
      const int wb = w * 8192;
      pc.inc = P1;
      STAMP(5);
      emit(30, k_part<true>(X, q2, pc, ldR(rkB, wb)));
      emit(31, k_part<true>(Y, q2, pc, ldR(rkB, wb + 32768)));
      lds_post(flag + 16 + w);
      lds_wait(flag + 1);
      v4u QB[4], PB[4];
#pragma unroll
      for (int k = 0; k < 4; ++k) QB[k] = *(const v4u*)(ldu + L_QB + 64 * c7 + 4 * (4 * k + g));
      unsigned* const tile = ldu + L_KT + w * 2048;
      PB[0] = k_part8(X, QB, tile, g, c, pc, ldR(rkB, wb + 2 * 32768));
      PB[1] = k_part8(Y, QB, tile, g, c, pc, ldR(rkB, wb + 3 * 32768));
      PB[2] = k_part8(X, QB, tile, g, c, pc, ldR(rvB, wb));
      PB[3] = k_part8(Y, QB, tile, g, c, pc, ldR(rvB, wb + 32768));
      float mB, lB = 0.f;
      {
        float e[32];
#pragma unroll
        for (int i = 0; i < 16; ++i) { e[2 * i] = u2f(PB[i >> 2][i & 3] << 16); e[2 * i + 1] = u2f(PB[i >> 2][i & 3] & 0xffff0000u); }
        mB = e[0];
#pragma unroll
        for (int i = 1; i < 32; ++i) mB = fmaxf(mB, e[i]);
        mB = xrow_max(mB);
#pragma unroll
        for (int i = 0; i < 32; ++i) { e[i] = __builtin_amdgcn_exp2f((e[i] - mB) * 1.4426950408889634f); lB += e[i]; }
#pragma unroll
        for (int i = 0; i < 16; ++i) PB[i >> 2][i & 3] = pk_bf16(e[2 * i], e[2 * i + 1]);
        lB = xrow_sum(lB);
        if (lane < 8) *(f32x2*)(lds + L_MLB + (w * 8 + lane) * 2) = f32x2{mB, lB};
      }
      acc_zero(acc);
      STAMP(6);
      v_part<true>(X, PB[0], acc, pc, ldR(rvB, wb + 2 * 32768));
      v_part<true>(Y, PB[1], acc, pc, ldR(rvB, wb + 3 * 32768));
      vb = (role ^ 1) * 128 + 2 * w + (lds_wait(flag + 3) - 1);
      pc.inc = P2;
      v_part<true>(X, PB[2], acc, pc, ldR(rvA, vb * 8192));
      v_part<true>(Y, PB[3], acc, pc, ldR(rvA, (vb + 8) * 8192));
#pragma unroll
      for (int k = 0; k < 4; ++k) *(f32x4*)(lds + L_RB + (w * 8 + c7) * RSTR + k * 32 + g * 8 + hi * 4) = tile_vec(acc, k, hi);
      lds_post(flag + 4 + w);
    }
    auto vblk = [&](int v) { return (vb + 8 * v) & 255; };
    auto prob = [&](int v) { return *(const v4u*)(sc + vblk(v) * 16); };
    auto dump_one = [&](int rows) {
      if (c7 == 0) {
#pragma unroll
        for (int k = 0; k < 4; ++k) *(f32x4*)(lds + rows + w * 128 + k * 32 + g * 8 + hi * 4) = tile_vec(acc, k, hi);
      }
    };
    acc_zero(acc);
    STAMP(7);
    lds_wait(flag + 24);
    STAMP(8);
    for (int v = 0; v < 16; v += 2) {
      v_part<true>(X, prob(v), acc, pc, ldR(rvA, vblk(v + 2) * 8192));
      v_part<true>(Y, prob(v + 1), acc, pc, ldR(rvA, vblk(v + 3) * 8192));
    }
    dump_one(L_RF);
    lds_post(flag + 8 + w);
    acc_zero(acc);
    lds_wait(flag + 25);
    for (int v = 16; v < 30; v += 2) {
      v_part<true>(X, prob(v), acc, pc, ldR(rvA, vblk(v + 2) * 8192));
      if (v == 28) flag[26] = 1u;
      v_part<true>(Y, prob(v + 1), acc, pc, ldR(rvA, vblk(v + 3) * 8192));
    }
    STAMP(10);
    const rsrc_t roa = mk_rsrc(p.woA, (size_t)(128 * h) * 2048 + 256 * b, 127 * 2048 + 256);
    const rsrc_t rob = mk_rsrc(p.woB, (size_t)(128 * h) * 2048 + 256 * b, 127 * 2048 + 256);
    const int wo = (32 * w + 8 * g) * 2048 + 16 * c;
    v4u K[8], V[8];
    {
      const v4u p30 = prob(30), p31 = prob(31);
      v_part<false>(X, p30, acc, pc, ldR(rvA, 0));
      PIN();
#pragma unroll
      for (int j = 0; j < 8; ++j) { K[j] = ld_nt(rob, wo, 2048 * j); SB(); }
      PIN();
      v_part<false>(Y, p31, acc, pc, ldR(rvA, 0));
    }
    STAMP(11);
    dump_one(L_RS);
    lds_post(flag + 13 + (w == 3 ? 10 : w));
    PIN();
#pragma unroll
    for (int j = 0; j < 8; ++j) { V[j] = ld_nt(roa, wo, 2048 * j); SB(); }
    PIN();
    STAMP(12);
    {
      f32x4 y[8];
      lds_wait(flag + 22);
      const f32x2 gt = *(const f32x2*)(lds + L_G + 2 * c7);
      f32x4 yv[4];
      {
        const v4u oB = *(const v4u*)(ldu + L_OB + 64 * c7 + 16 * w + 4 * g);
        v4u a[8];
#pragma unroll
        for (int q = 0; q < 4; ++q) tr_pair(K[2 * q], K[2 * q + 1], a, q);
#pragma unroll
        for (int e = 0; e < 8; ++e) y[e] = mfma(a[e], oB, f32x4{0.f, 0.f, 0.f, 0.f});
#pragma unroll
        for (int k = 0; k < 4; ++k) yv[k] = gt.y * tile_vec(y, k, hi);
      }
      v4u a[8];
#pragma unroll
      for (int q = 0; q < 4; ++q) tr_pair(V[2 * q], V[2 * q + 1], a, q);
      asm volatile("" : "+v"(a[0]), "+v"(a[1]), "+v"(a[2]), "+v"(a[3]), "+v"(a[4]), "+v"(a[5]), "+v"(a[6]), "+v"(a[7]), "+v"(yv[0]), "+v"(yv[1]), "+v"(yv[2]), "+v"(yv[3]) :: "memory");
      {
        v4u u[2];
        const int oa = WS_O + ((h * 8 + c7) * 128 + 32 * w + 8 * g) * 4;
        poll<0xffu, 2>(rws, [&](int k) { return oa + 16 * k; }, u, T);
        const v2u o0 = pack_hi(u[0]), o1 = pack_hi(u[1]);
        const v4u oA = {o0.x, o0.y, o1.x, o1.y};
#pragma unroll
        for (int e = 0; e < 8; ++e) y[e] = mfma(a[e], oA, f32x4{0.f, 0.f, 0.f, 0.f});
      }
      STAMP(13);
#pragma unroll
      for (int k = 0; k < 4; ++k) *(f32x4*)(lds + L_RY + ((w * 4 + k) * 64 + lane) * 4) = yv[k] + gt.x * tile_vec(y, k, hi);
      wg_barrier();
      f32x4 sum = *(const f32x4*)(lds + L_RY + (w * 64 + lane) * 4);
#pragma unroll
      for (int ww = 1; ww < 4; ++ww) sum += *(const f32x4*)(lds + L_RY + ((ww * 4 + w) * 64 + lane) * 4);
      st_sc1(rws, WS_Y + (((b * 32 + c7 * 4 + w) * 32 + h) * 32 + g * 8 + hi * 4) * 4, tag_f32<0xffu>(sum, T));
    }
    STAMP(14);
    if (w == 0) {
      drain();
      v4u u[4];
      const int yb = WS_Y + me * 4096 + lane * 16;
      poll<0xffu, 4>(rws, [&](int k) { return yb + 1024 * k; }, u, T);
      f32x4 sum = (untag<0xffu>(u[0]) + untag<0xffu>(u[1])) + (untag<0xffu>(u[2]) + untag<0xffu>(u[3]));
#pragma unroll
      for (int i = 0; i < 4; ++i) sum[i] = xrow_sum(sum[i] + dpp<0x128>(sum[i]));
      const rsrc_t ro = mk_rsrc(p.out, 0, 8 * 1024 * 2);
      if (lane < 8) {
        const v2u o = {pk_bf16(sum[0], sum[1]), pk_bf16(sum[2], sum[3])};
        __builtin_amdgcn_raw_buffer_store_b64(o, ro, ((h >> 2) * 1024 + 128 * b + tile_n0(h & 3, lane >> 1, lane & 1)) * 2, 0, 0);
      }
      if (blockIdx.x == 0 && lane == 0) __builtin_amdgcn_raw_buffer_store_b32(T, rws, WS_EPOCH, 0, AUX_SC1);
    }
    STAMP(15);
    while ((unsigned)__builtin_amdgcn_s_memrealtime() - dg0 < 15000 + (dgt - dg0)) __builtin_amdgcn_s_sleep(1);
  }
}

extern "C" void kernel_launch(void* const* d_in, const int* in_sizes, int n_in, void* d_out, int out_size,
                              void* d_ws, size_t ws_size, hipStream_t stream) {
  Params p;
  p.x = d_in[0]; p.kA = d_in[1]; p.vA = d_in[2]; p.wqA = d_in[3]; p.woA = d_in[4];
  p.kB = d_in[6]; p.vB = d_in[7]; p.wqB = d_in[8]; p.woB = d_in[9]; p.gate = d_in[10];
  p.out = d_out; p.ws = d_ws;
  hipLaunchKernelGGL(fused_decode, dim3(256), dim3(512), 0, stream, p);
}
```

```cpp
#include <hip/hip_runtime.h>
#include <stdint.h>

#define WIN 8
#define P1 14400
#define PBB 15600
#define P2 13650
#define P3 13750
#define PW 14400
#define ODELAY -1
#define YDELAY -1
#define OVER 0
#define PT0 (330 * 256)
#define SLIP 1100
#define CTRL 1
#define CP2 5200
#define CPD 900
#define NCP 3
#define CWAVE 0
#define NPK 9
#define NPV 17
#define QDELAY 100
#define HX 3
#define FORCE -1

typedef unsigned v4u __attribute__((ext_vector_type(4)));
typedef unsigned v2u __attribute__((ext_vector_type(2)));
typedef float f32x4 __attribute__((ext_vector_type(4)));
typedef float f32x2 __attribute__((ext_vector_type(2)));
typedef __bf16 bf16x8 __attribute__((ext_vector_type(8)));
typedef __bf16 bf16x2 __attribute__((ext_vector_type(2)));
typedef __amdgpu_buffer_rsrc_t rsrc_t;

constexpr int AUX_NT = 2, AUX_SC1 = 16;
constexpr int OOB = 0x40000000;
constexpr int WS_EPOCH = 0;
constexpr int WS_QPART = 4096;
constexpr int WS_QB = WS_QPART + 2 * 32 * 8 * 8 * 128 * 4;
constexpr int WS_BACC = WS_QB + 8 * 32 * 128 * 4;
constexpr int WS_BML = WS_BACC + 8 * 32 * 8 * 128 * 4;
constexpr int WS_O = WS_BML + 8 * 32 * 8 * 4 * 4;
constexpr int WS_G = WS_O + 2 * 32 * 8 * 128 * 4;
constexpr int WS_Y = WS_G + 32 * 8 * 4 * 4;
constexpr int WS_AP = WS_Y + 8 * 32 * 32 * 32 * 4;
constexpr int WS_PR = WS_AP + 256 * 320 * 4;
constexpr int WS_IN = WS_PR + 256 * 4 * 32 * 4;
constexpr int WS_LAG = WS_IN + 256 * 8192;
constexpr int WS_END = WS_LAG + 8 * 4096;
constexpr int RSTR = 132;
constexpr unsigned SPIN_CAP = 1u << 20;
constexpr unsigned TAG_INC = 0x9E3579B1u;

#define STAMP(i) do {} while (0)

struct Params {
  const void* x; const void* kA; const void* vA; const void* wqA; const void* woA;
  const void* kB; const void* vB; const void* wqB; const void* woB; const void* gate;
  void* out; void* ws;
};

__device__ __forceinline__ rsrc_t mk_rsrc(const void* p, size_t off, int bytes) {
  return __builtin_amdgcn_make_buffer_rsrc((void*)((const char*)p + off), 0, bytes, 0x00020000);
}
__device__ __forceinline__ v4u ld_nt(rsrc_t r, int voff, int soff) { return __builtin_amdgcn_raw_buffer_load_b128(r, voff, soff, AUX_NT); }
__device__ __forceinline__ v4u ld_plain(rsrc_t r, int voff, int soff) { return __builtin_amdgcn_raw_buffer_load_b128(r, voff, soff, 0); }
__device__ __forceinline__ v4u ld_sc1(rsrc_t r, int voff, int soff = 0) { return __builtin_amdgcn_raw_buffer_load_b128(r, voff, soff, AUX_SC1); }
__device__ __forceinline__ void st_sc1(rsrc_t r, int voff, v4u v, int soff = 0) { __builtin_amdgcn_raw_buffer_store_b128(v, r, voff, soff, AUX_SC1); }
template <int N> __device__ __forceinline__ void wait_vm() {
  __builtin_amdgcn_s_waitcnt((N & 15) | ((N >> 4) << 14) | (7 << 4) | (15 << 8));
}
__device__ __forceinline__ void drain() { asm volatile("s_waitcnt vmcnt(0)" ::: "memory"); }
__device__ __forceinline__ void wg_barrier() { asm volatile("s_waitcnt lgkmcnt(0)\n\ts_barrier" ::: "memory"); }
#define PIN() do { asm volatile("" ::: "memory"); __builtin_amdgcn_sched_barrier(0); } while (0)
#define SB() __builtin_amdgcn_sched_barrier(0)

__device__ __forceinline__ float u2f(unsigned u) { return __builtin_bit_cast(float, u); }
__device__ __forceinline__ unsigned f2u(float f) { return __builtin_bit_cast(unsigned, f); }
__device__ __forceinline__ unsigned pk_bf16(float lo, float hi) {
  f32x2 v = {lo, hi};
  return __builtin_bit_cast(unsigned, __builtin_convertvector(v, bf16x2));
}
__device__ __forceinline__ float rnd_bf16(float x) { return u2f(pk_bf16(x, x) & 0xffff0000u); }
__device__ __forceinline__ f32x4 mfma(v4u a, v4u b, f32x4 c) {
  return __builtin_amdgcn_mfma_f32_16x16x32_bf16(__builtin_bit_cast(bf16x8, a), __builtin_bit_cast(bf16x8, b), c, 0, 0, 0);
}
__device__ __forceinline__ float dot2(unsigned a, unsigned b, float c) {
  return __builtin_amdgcn_fdot2_f32_bf16(__builtin_bit_cast(bf16x2, a), __builtin_bit_cast(bf16x2, b), c, false);
}
template <int DC> __device__ __forceinline__ float dpp(float x) {
  return u2f((unsigned)__builtin_amdgcn_mov_dpp((int)f2u(x), DC, 0xf, 0xf, true));
}
__device__ __forceinline__ float row_allsum(float x) {
  x += dpp<0x128>(x); x += dpp<0x124>(x); x += dpp<0x122>(x); x += dpp<0x121>(x); return x;
}
__device__ __forceinline__ float xrow_max(float x) {
  auto s = __builtin_amdgcn_permlane16_swap(f2u(x), f2u(x), false, false);
  x = fmaxf(u2f(s[0]), u2f(s[1]));
  auto t = __builtin_amdgcn_permlane32_swap(f2u(x), f2u(x), false, false);
  return fmaxf(u2f(t[0]), u2f(t[1]));
}
__device__ __forceinline__ float half_sum(float x) {
  auto t = __builtin_amdgcn_permlane32_swap(f2u(x), f2u(x), false, false);
  return u2f(t[0]) + u2f(t[1]);
}
__device__ __forceinline__ float xrow_sum(float x) {
  auto s = __builtin_amdgcn_permlane16_swap(f2u(x), f2u(x), false, false);
  return half_sum(u2f(s[0]) + u2f(s[1]));
}
template <unsigned M> __device__ __forceinline__ v4u tag_raw(v4u v, unsigned T) {
  v4u r;
  r.x = (v.x & ~M) | (T & M);
  r.y = (v.y & ~M) | ((T >> 8) & M);
  r.z = (v.z & ~M) | ((T >> 16) & M);
  r.w = (v.w & ~M) | ((T >> 24) & M);
  return r;
}
template <unsigned M> __device__ __forceinline__ v4u tag_f32(f32x4 v, unsigned T) {
  constexpr unsigned H = (M + 1) / 2;
  v4u u = {f2u(v.x) + H, f2u(v.y) + H, f2u(v.z) + H, f2u(v.w) + H};
  return tag_raw<M>(u, T);
}
__device__ __forceinline__ v4u tag_bf16(f32x4 v, unsigned T) {
  const unsigned a = pk_bf16(v.x, v.y), b = pk_bf16(v.z, v.w);
  v4u u = {a << 16, a & 0xffff0000u, b << 16, b & 0xffff0000u};
  return tag_raw<0xffu>(u, T);
}
template <unsigned M> __device__ __forceinline__ bool tag_ok(v4u v, unsigned T) {
  const unsigned lo = __builtin_amdgcn_perm(v.y, v.x, 0x0c0c0400u), hi = __builtin_amdgcn_perm(v.w, v.z, 0x04000c0cu);
  return (((lo | hi) ^ T) & (M * 0x01010101u)) == 0u;
}
template <unsigned M> __device__ __forceinline__ f32x4 untag(v4u v) {
  f32x4 r = {u2f(v.x & ~M), u2f(v.y & ~M), u2f(v.z & ~M), u2f(v.w & ~M)};
  return r;
}
__device__ __forceinline__ v2u pack_hi(v4u a) {
  return v2u{__builtin_amdgcn_perm(a.y, a.x, 0x07060302u), __builtin_amdgcn_perm(a.w, a.z, 0x07060302u)};
}
template <unsigned M, int N, int SLP = 2, class F> __device__ __forceinline__ void poll(rsrc_t ws, F off, v4u (&v)[N], unsigned T) {
  for (unsigned spin = 0;; ++spin) {
    asm volatile("" ::: "memory");
#pragma unroll
    for (int k = 0; k < N; ++k) v[k] = ld_sc1(ws, off(k));
    bool ok = true;
#pragma unroll
    for (int k = 0; k < N; ++k) ok &= tag_ok<M>(v[k], T);
    if (__builtin_amdgcn_ballot_w64(!ok) == 0ull || spin > SPIN_CAP) break;
    __builtin_amdgcn_s_sleep(SLP);
  }
}
__device__ __forceinline__ void tr_pair(const v4u& r0, const v4u& r1, v4u (&a)[8], int p) {
#pragma unroll
  for (int d = 0; d < 4; ++d) {
    a[2 * d][p] = __builtin_amdgcn_perm(r1[d], r0[d], 0x05040100u);
    a[2 * d + 1][p] = __builtin_amdgcn_perm(r1[d], r0[d], 0x07060302u);
  }
}
__device__ __forceinline__ f32x4 tile_vec(const f32x4 (&acc)[8], int k, bool hi) {
  f32x4 r;
#pragma unroll
  for (int e = 0; e < 4; ++e) r[e] = hi ? acc[4 * (k & 1) + e][2 * (k >> 1) + 1] : acc[4 * (k & 1) + e][2 * (k >> 1)];
  return r;
}
__device__ __forceinline__ int tile_n0(int k, int g, int hi) { return 32 * g + 16 * (k >> 1) + 8 * hi + 4 * (k & 1); }

typedef volatile __attribute__((address_space(3))) unsigned lflag_t;
struct Pacer {
  unsigned t0; int acc, inc, off, goal;
  lflag_t* mine; lflag_t* cmd;
  template <bool FIRST> __device__ __forceinline__ void tick() {
    unsigned gl;
    if (FIRST && CTRL) gl = *cmd;
    int t;
    while ((t = (int)((unsigned)__builtin_amdgcn_s_memrealtime() - t0)) < ((off + acc) >> 8)) __builtin_amdgcn_s_sleep(1);
    if (FIRST && CTRL) {
      const int e = t - (acc >> 8);
      *mine = (unsigned)e; goal = __builtin_amdgcn_readfirstlane(gl);
      off = max(off, (e - 8) << 8);
    }
    acc += inc;
    if (CTRL) off = min(goal, off + SLIP);
  }
};
__device__ __forceinline__ unsigned round_scores(float a, float b) {
  const unsigned u = pk_bf16(a, b);
  return pk_bf16(u2f(u << 16) * 0.08838834764831845f, u2f(u & 0xffff0000u) * 0.08838834764831845f);
}
template <bool RF, class F>
__device__ __forceinline__ v4u k_part(v4u (&R)[8], const v4u& q, Pacer& pc, F nx) {
  float s[8];
#pragma unroll
  for (int j = 0; j < 8; ++j) {
    float d = dot2(R[j].x, q.x, 0.f);
    d = dot2(R[j].y, q.y, d); d = dot2(R[j].z, q.z, d); d = dot2(R[j].w, q.w, d);
    asm volatile("" : "+v"(d) :: "memory");
    s[j] = d;
    SB();
    if constexpr (RF) { if (j == 0) pc.tick<true>(); if (j == 4) pc.tick<false>(); wait_vm<WIN - 1>(); R[j] = nx(j); }
    SB();
  }
#pragma unroll
  for (int j = 0; j < 8; ++j) s[j] = row_allsum(s[j]);
  return v4u{round_scores(s[0], s[1]), round_scores(s[2], s[3]), round_scores(s[4], s[5]), round_scores(s[6], s[7])};
}
template <class F>
__device__ __forceinline__ v4u k_part8(v4u (&R)[8], const v4u (&Q)[4], unsigned* tile, int g, int c, Pacer& pc, F nx) {
#pragma unroll
  for (int j = 0; j < 8; ++j) {
    *(v4u*)(tile + (4 * j + g) * 64 + (c ^ (4 * g + (j & 3))) * 4) = R[j];
    PIN();
    if (j == 0) pc.tick<true>();
    if (j == 4) pc.tick<false>();
    wait_vm<WIN - 1>(); R[j] = nx(j);
    SB();
  }
  f32x4 d[2];
#pragma unroll
  for (int t = 0; t < 2; ++t) {
    d[t] = f32x4{0.f, 0.f, 0.f, 0.f};
#pragma unroll
    for (int k = 0; k < 4; ++k) d[t] = mfma(*(const v4u*)(tile + (16 * t + 4 * (c & 3) + (c >> 2)) * 64 + ((4 * k + g) ^ c) * 4), Q[k], d[t]);
  }
  v4u r = {round_scores(d[0][0], d[0][1]), round_scores(d[0][2], d[0][3]), round_scores(d[1][0], d[1][1]), round_scores(d[1][2], d[1][3])};
  asm volatile("" : "+v"(r) :: "memory");
  return r;
}
template <bool RF, class F>
__device__ __forceinline__ void v_part(v4u (&R)[8], const v4u& P, f32x4 (&acc)[8], Pacer& pc, F nx) {
  v4u a[8];
#pragma unroll
  for (int p = 0; p < 4; ++p) {
    tr_pair(R[2 * p], R[2 * p + 1], a, p);
    asm volatile("" : "+v"(a[0][p]), "+v"(a[1][p]), "+v"(a[2][p]), "+v"(a[3][p]), "+v"(a[4][p]), "+v"(a[5][p]), "+v"(a[6][p]), "+v"(a[7][p]) :: "memory");
    SB();
    if constexpr (RF) {
      if (p == 0) pc.tick<true>();
      if (p == 2) pc.tick<false>();
      wait_vm<WIN - 1>(); R[2 * p] = nx(2 * p);
      SB();
      wait_vm<WIN - 1>(); R[2 * p + 1] = nx(2 * p + 1);
    }
    SB();
  }
#pragma unroll
  for (int e = 0; e < 8; ++e) acc[e] = mfma(a[e], P, acc[e]);
}
__device__ __forceinline__ void acc_zero(f32x4 (&acc)[8]) {
#pragma unroll
  for (int e = 0; e < 8; ++e) acc[e] = f32x4{0.f, 0.f, 0.f, 0.f};
}
__device__ __forceinline__ int blind_block(int n, int w) { return 8 * (n >> 1) + 4 * (w >> 1) + 2 * (n & 1) + (w & 1); }
__device__ __forceinline__ int k_block(int n, int w, int r, int par) {
  return n < 8 ? r * 128 + blind_block(n, w) : (n < 20 ? r * 128 - 64 : (r ^ 1) * 128 - 160) + 32 + 16 * (n >> 1) + 4 * w + 2 * (n & 1) + par;
}

constexpr int L_FLAG = 0;
constexpr int L_T = 31;
constexpr int L_T0 = 40;
constexpr int L_QA = 64;
constexpr int L_QB = L_QA + 128;
constexpr int L_OB = L_QB + 512;
constexpr int L_OA = L_OB + 512;
constexpr int L_G = L_OA + 512;
constexpr int L_MLB = L_G + 16;
constexpr int L_RB = L_MLB + 64;
constexpr int L_RF = L_RB + 4 * 8 * RSTR;
constexpr int L_RS = L_RF + 4 * 128;
constexpr int L_RY = L_RS + 4 * 128;
constexpr int L_SC = L_RY + 4096;
constexpr int L_KT = L_SC + 4096;
constexpr int L_END = L_KT + 4 * 2048;
constexpr int L_WORDS = L_END;
static_assert(L_WORDS * 4 > 81920 && L_WORDS * 4 <= 160 * 1024, "lds");

__device__ __forceinline__ unsigned lds_wait(lflag_t* f) {
  unsigned v;
  for (unsigned spin = 0; (v = __builtin_amdgcn_readfirstlane(*f)) == 0u && spin < (SPIN_CAP << 4); ++spin) __builtin_amdgcn_s_sleep(2);
  asm volatile("" ::: "memory");
  return v;
}
__device__ __forceinline__ void lds_wait_gt(lflag_t* f, unsigned n) {
  for (unsigned spin = 0; (unsigned)__builtin_amdgcn_readfirstlane(*f) <= n && spin < (SPIN_CAP << 4); ++spin) __builtin_amdgcn_s_sleep(2);
  asm volatile("" ::: "memory");
}
__device__ __forceinline__ unsigned now() { return (unsigned)__builtin_amdgcn_s_memrealtime(); }
__device__ __forceinline__ void sleep_until(unsigned t) { while ((int)(now() - t) < 0) __builtin_amdgcn_s_sleep(2); }
__device__ __forceinline__ void lds_post(lflag_t* f, unsigned v = 1u) {
  asm volatile("s_waitcnt lgkmcnt(0)" ::: "memory");
  *f = v;
}
template <int NR> __device__ __forceinline__ void merge_recs(const float* ml, int mlstr, const float* rows, int rstr, float& M, float& L, f32x4& sum) {
  constexpr float LOG2E = 1.4426950408889634f;
  f32x2 r[NR];
#pragma unroll
  for (int k = 0; k < NR; ++k) r[k] = *(const f32x2*)(ml + k * mlstr);
  M = r[0].x;
#pragma unroll
  for (int k = 1; k < NR; ++k) M = fmaxf(M, r[k].x);
  sum = f32x4{0.f, 0.f, 0.f, 0.f}; L = 0.f;
#pragma unroll
  for (int k = 0; k < NR; ++k) {
    const float wt = __builtin_amdgcn_exp2f((r[k].x - M) * LOG2E);
    L = fmaf(wt, r[k].y, L);
    sum += wt * *(const f32x4*)(rows + k * rstr);
  }
}

__global__ void __launch_bounds__(512, 2) fused_decode(Params p) {
  __shared__ __attribute__((aligned(16))) float lds[L_WORDS];
  const int tid = threadIdx.x, lane = tid & 63, w = __builtin_amdgcn_readfirstlane(tid >> 6) ^ 4;
  const int g = lane >> 4, c = lane & 15, c7 = lane & 7, hi = c >> 3;
  const int b = blockIdx.x >> 5;
  const int h = (blockIdx.x & 26) | ((blockIdx.x & 1) << 2) | (((blockIdx.x >> 2) ^ (blockIdx.x >> HX)) & 1);
  const int me = b * 32 + h, role = h & 1;
  unsigned* const ldu = (unsigned*)lds;
  lflag_t* const flag = (lflag_t*)lds + L_FLAG;
  const rsrc_t rws = mk_rsrc(p.ws, 0, WS_END);
  const int rec_n0 = tile_n0((lane >> 3) & 3, (lane >> 1) & 3, lane & 1);
  STAMP(0);
  const unsigned tstart = now();
  unsigned cwarm;
  {
    unsigned long long pc0; int len;
    asm volatile("s_getpc_b64 %0\n.Lfd_code_begin:\n\ts_mov_b32 %1, .Lfd_code_end-.Lfd_code_begin" : "=s"(pc0), "=s"(len));
    cwarm = __builtin_amdgcn_raw_buffer_load_b32(mk_rsrc((const void*)pc0, 0, w == CWAVE ? len : 0),
                                                 lane < 32 ? (lane * 32 + (blockIdx.x >> 3)) * 64 : OOB, 0, 0);
    PIN();
  }
  if (tid < 48) ldu[tid] = tid == L_T0 ? tstart : 0u;
  wg_barrier();

  if (__builtin_expect(w >= 6, 0)) {
    const unsigned ep = __builtin_amdgcn_raw_buffer_load_b32(rws, WS_EPOCH, 0, AUX_SC1);
    const unsigned rnd = (unsigned)__builtin_amdgcn_s_memtime();
    auto probe = [&](const void* tensor, int k, int par) -> unsigned {
      const char* a = (const char*)tensor + ((size_t)me << 20) + (32 + ((rnd + 7 * k) & 31)) * 16384 + par * 8192 + (((rnd >> 5) + 13 * k) & 63) * 128;
      unsigned long long t0, t1; unsigned v;
      asm volatile("s_memtime %0\n\ts_waitcnt lgkmcnt(0)\n\ts_load_dword %2, %3, 0x0\n\ts_waitcnt lgkmcnt(0)\n\ts_memtime %1\n\ts_waitcnt lgkmcnt(0)"
                   : "=&s"(t0), "=&s"(t1), "=&s"(v) : "s"(a) : "memory");
      const unsigned dt = (unsigned)(t1 - t0);
      return dt < 520u ? 4000u : dt;
    };
    unsigned m0 = 4000u, m1 = 4000u;
    for (int k = 0; k < NPK; ++k) m0 = min(m0, probe(p.kA, k, w - 6));
    if (w == 7) {
      lds_post(flag + 20, m0);
      m0 = 4000u;
      for (int k = 0; k < NPV; ++k) { const unsigned dt = probe(p.vA, k, k & 1); if (k & 1) m1 = min(m1, dt); else m0 = min(m0, dt); }
    } else m1 = lds_wait(flag + 20);
    STAMP(1);
    const unsigned T = (unsigned)__builtin_amdgcn_readfirstlane(ep) + TAG_INC;
    const unsigned d = m0 + 4096u - m1;
    auto swap_word = [&](int slot, unsigned val) -> unsigned {
      if (lane == 0) st_sc1(rws, WS_PR + ((me ^ 1) * 4 + slot) * 128, v4u{(val << 8) | (T & 0xffu), T, ~T, T});
      drain();
      sleep_until(now() + 40);
      v4u u;
      for (unsigned spin = 0; spin <= SPIN_CAP; ++spin) {
        asm volatile("" ::: "memory");
        u = ld_sc1(rws, WS_PR + (me * 4 + slot) * 128);
        if (__builtin_amdgcn_readfirstlane(((u.x ^ T) & 0xffu) | (u.y ^ T) | (u.z ^ ~T) | (u.w ^ T)) == 0u) break;
        __builtin_amdgcn_s_sleep(16);
      }
      return (unsigned)__builtin_amdgcn_readfirstlane(u.x) >> 8;
    };
    auto consensus = [&](int k, unsigned tick) {
      const unsigned t0 = (unsigned)__builtin_amdgcn_readfirstlane(ldu[L_T0]);
      sleep_until(t0 + tick);
      const unsigned e = t0 + ((flag[32] + flag[33] + flag[34] + flag[35]) >> 2);
      if (lane == 0) st_sc1(rws, WS_LAG + k * 4096 + me * 16, tag_raw<0xffu>(v4u{e << 8, 0u, 0u, 0u}, T));
      drain();
      sleep_until(now() + 100);
      v4u u[4];
      poll<0xffu, 4, 32>(rws, [&](int i) { return WS_LAG + k * 4096 + i * 1024 + lane * 16; }, u, T);
      float sum = 0.f;
#pragma unroll
      for (int i = 0; i < 4; ++i) sum += (float)((int)((u[i].x & 0xffffff00u) - (t0 << 8)) >> 8);
      sum = xrow_sum(row_allsum(sum));
      flag[36] = (unsigned)min(max((int)sum - 4 * 256 + (k == NCP + 1 ? OVER * 256 : 0), -200 * 256), 3000 * 256);
    };
    const unsigned dp = swap_word(w - 6, d);
    unsigned par = (role ? dp < d : d < dp) ? 0u : 1u;
    if (FORCE >= 0) par = (FORCE >> (w - 6)) & 1;
    par ^= role;
    lds_post(flag + (w - 4), 1u + par);
    STAMP(2);
    if (w == 6) {
      if (CTRL) {
#pragma nounroll
        for (int k = 0; k < 2; ++k) consensus(k, 1400 + 1200 * k); }
      sleep_until(tstart + 3600);
      lds_wait(flag + 16); lds_wait(flag + 17); lds_wait(flag + 18); lds_wait(flag + 19);
      STAMP(3);
#pragma unroll
      for (int k = 0; k < 8; ++k)
        st_sc1(rws, WS_IN + (me ^ 1) * 8192 + k * 1024 + lane * 16, *(const v4u*)(ldu + L_SC + k_block(4 * k + g, c >> 2, role, par) * 16 + (c & 3) * 4));
      drain();
      swap_word(2, 0u);
      __builtin_amdgcn_fence(__ATOMIC_ACQUIRE, "wavefront");
      v4u x[8];
#pragma unroll
      for (int k = 0; k < 8; ++k) x[k] = ld_sc1(rws, WS_IN + me * 8192 + k * 1024 + lane * 16);
#pragma unroll
      for (int k = 0; k < 8; ++k) *(v4u*)(ldu + L_SC + k_block(4 * k + g, c >> 2, role ^ 1, par ^ 1) * 16 + (c & 3) * 4) = x[k];
      lds_post(flag + 21);
      STAMP(4);
    } else {
      sleep_until(tstart + 4000);
      lds_wait(flag + 21);
    }
    {
      unsigned* tb = ldu + L_SC + ((role ^ (w & 1) ^ 1) * 128) * 16 + lane * 4;
      float e[64];
#pragma unroll
      for (int k = 0; k < 8; ++k) {
        const v4u x = *(const v4u*)(tb + k * 256);
#pragma unroll
        for (int i = 0; i < 4; ++i) { e[8 * k + 2 * i] = u2f(x[i] << 16); e[8 * k + 2 * i + 1] = u2f(x[i] & 0xffff0000u); }
      }
      float M = e[0];
#pragma unroll
      for (int i = 1; i < 64; ++i) M = fmaxf(M, e[i]);
      M = fmaxf(M, dpp<0x128>(M)); M = fmaxf(M, dpp<0x124>(M)); M = fmaxf(M, dpp<0x122>(M)); M = fmaxf(M, dpp<0x121>(M));
      M = xrow_max(M);
      float L = 0.f;
#pragma unroll
      for (int i = 0; i < 64; ++i) { e[i] = __builtin_amdgcn_exp2f((e[i] - M) * 1.4426950408889634f); L += e[i]; }
      L = 1.f / xrow_sum(row_allsum(L));
#pragma unroll
      for (int k = 0; k < 8; ++k)
        *(v4u*)(tb + k * 256) = v4u{pk_bf16(e[8 * k] * L, e[8 * k + 1] * L), pk_bf16(e[8 * k + 2] * L, e[8 * k + 3] * L),
                                    pk_bf16(e[8 * k + 4] * L, e[8 * k + 5] * L), pk_bf16(e[8 * k + 6] * L, e[8 * k + 7] * L)};
      lds_post(flag + 18 + w);
    }
    STAMP(5);
    if (CTRL && w == 7) {
#pragma nounroll
      for (int k = 0; k < NCP; ++k) consensus(2 + k, CP2 + CPD * k); }
  } else if (__builtin_expect(w >= 4, 1)) {
    __builtin_amdgcn_s_setprio(3);
    const int mat = w - 4;
    const rsrc_t rx = mk_rsrc(p.x, 0, 8 * 1024 * 2);
    STAMP(9);
    if (mat) __builtin_amdgcn_s_sleep(8);
    const unsigned ep = __builtin_amdgcn_raw_buffer_load_b32(rws, WS_EPOCH, 0, AUX_SC1);
    v4u xf[4], W[32];
#pragma unroll
    for (int kb = 0; kb < 4; ++kb) xf[kb] = ld_plain(rx, c7 * 2048 + (128 * b + 32 * kb + 8 * g) * 2, 0);
    {
      const rsrc_t rq = mk_rsrc(mat ? p.wqB : p.wqA, (size_t)(128 * b) * 8192 + 256 * h, 127 * 8192 + 256);
      const int vo = 8 * g * 8192 + 16 * c;
#pragma unroll
      for (int j = 0; j < 32; ++j) { W[j] = ld_nt(rq, vo, 8192 * (32 * (j >> 3) + (j & 7))); SB(); if (j == 15) STAMP(10); }
      PIN();
      STAMP(11);
    }
    const unsigned T = (unsigned)__builtin_amdgcn_readfirstlane(ep) + TAG_INC;
    {
      f32x4 d[8];
#pragma unroll
      for (int e = 0; e < 8; ++e) d[e] = f32x4{0.f, 0.f, 0.f, 0.f};
#pragma unroll
      for (int kb = 0; kb < 4; ++kb) {
        v4u a[8];
#pragma unroll
        for (int q = 0; q < 4; ++q) tr_pair(W[8 * kb + 2 * q], W[8 * kb + 2 * q + 1], a, q);
#pragma unroll
        for (int e = 0; e < 8; ++e) d[e] = mfma(a[e], xf[kb], d[e]);
        SB();
      }
      STAMP(1);
      const int vo = WS_QPART + ((((mat * 32 + h) * 8 + c7) * 8 + b) * 128 + g * 8 + hi * 4) * 4;
#pragma unroll
      for (int k = 0; k < 4; ++k) st_sc1(rws, vo + 128 * k, tag_f32<3u>(tile_vec(d, k, hi), T));
    }
    STAMP(2);
    drain();
    auto gather_q = [&](int head) -> f32x4 {
      v4u u[4];
      const int base = WS_QPART + ((mat * 32 + head) * 8 + b) * 4096 + lane * 16;
      poll<3u, 4, 16>(rws, [&](int k) { return base + 1024 * k; }, u, T);
      f32x4 q = (untag<3u>(u[0]) + untag<3u>(u[1])) + (untag<3u>(u[2]) + untag<3u>(u[3]));
#pragma unroll
      for (int i = 0; i < 4; ++i) q[i] = half_sum(q[i]);
      return q;
    };
    sleep_until(now() + (mat ? 400 : QDELAY));
    if (mat == 0) {
      const f32x4 qs = gather_q(h);
      if (lane < 32) *(v2u*)(ldu + L_QA + rec_n0 / 2) = v2u{pk_bf16(qs[0], qs[1]), pk_bf16(qs[2], qs[3])};
      if (lane == 0) ldu[L_T] = T;
      lds_post(flag + 0, 1u);
      STAMP(3);
      __builtin_amdgcn_s_setprio(0);
      sleep_until(now() + 300);
      const f32x4 qf = gather_q(h ^ 1);
      if (lane < 32) *(v2u*)(ldu + L_QA + 64 + rec_n0 / 2) = v2u{pk_bf16(qf[0], qf[1]), pk_bf16(qf[2], qf[3])};
      lds_post(flag + 12, 1u);
      sleep_until(tstart + 6000);
      lds_wait(flag + 8); lds_wait(flag + 9); lds_wait(flag + 10); lds_wait(flag + 11);
      STAMP(4);
      auto sum4 = [&](int rows) {
        const float* r = lds + rows + 4 * (lane & 31);
        return (*(const f32x4*)(r) + *(const f32x4*)(r + 128)) + (*(const f32x4*)(r + 256) + *(const f32x4*)(r + 384));
      };
      {
        const f32x4 sum = sum4(L_RF);
        const f32x4 hi4 = untag<0xffu>(__builtin_bit_cast(v4u, sum));
        st_sc1(rws, WS_AP + (me ^ 1) * 1280 + lane * 16, tag_raw<0xffu>(__builtin_bit_cast(v4u, lane < 32 ? hi4 : sum - hi4), T));
      }
      sleep_until(now() + 500);
      f32x4 rp;
      {
        v4u u[1];
        poll<0xffu, 1, 64>(rws, [&](int) { return WS_AP + me * 1280 + lane * 16; }, u, T);
        rp = untag<0xffu>(u[0]);
#pragma unroll
        for (int i = 0; i < 4; ++i) rp[i] = half_sum(rp[i]);
      }
      STAMP(5);
      lds_wait(flag + 26);
      __builtin_amdgcn_s_setprio(3);
      const int oo = lane < 32 ? WS_O + ((h * 8 + b) * 128 + rec_n0) * 4 : OOB;
      for (unsigned spin = 0; spin < (SPIN_CAP << 4); ++spin) {
        const unsigned f = flag[13] & flag[14] & flag[15] & flag[23];
        if (__builtin_amdgcn_readfirstlane(f)) break;
      }
      asm volatile("" ::: "memory");
      STAMP(6);
      st_sc1(rws, oo, tag_bf16(sum4(L_RS) + rp, T));
      STAMP(7);
      if (ODELAY < 0) drain(); else sleep_until(now() + ODELAY);
      {
        v4u u[4];
        poll<0xffu, 4, 1>(rws, [&](int k) { return WS_O + h * 4096 + 1024 * k + lane * 16; }, u, T);
#pragma unroll
        for (int k = 0; k < 4; ++k) *(v2u*)(ldu + L_OA + 128 * k + 2 * lane) = pack_hi(u[k]);
        lds_post(flag + 27);
      }
      STAMP(8);
    } else {
      {
        const f32x4 q = gather_q(h);
        if (lane < 32) st_sc1(rws, WS_QB + (me * 128 + rec_n0) * 4, tag_bf16(q, T));
      }
      {
        const rsrc_t rg = mk_rsrc(p.gate, 0, 2048);
        const v4u x0 = ld_plain(rx, b * 2048 + lane * 32, 0), x1 = ld_plain(rx, b * 2048 + lane * 32 + 16, 0);
        const v4u g0 = ld_plain(rg, lane * 32, 0), g1 = ld_plain(rg, lane * 32 + 16, 0);
        float gs = 0.f;
#pragma unroll
        for (int i = 0; i < 4; ++i) {
          gs += rnd_bf16(u2f(x0[i] << 16) * u2f(g0[i] << 16)) + rnd_bf16(u2f(x0[i] & 0xffff0000u) * u2f(g0[i] & 0xffff0000u));
          gs += rnd_bf16(u2f(x1[i] << 16) * u2f(g1[i] << 16)) + rnd_bf16(u2f(x1[i] & 0xffff0000u) * u2f(g1[i] & 0xffff0000u));
        }
        gs = rnd_bf16(xrow_sum(row_allsum(gs)));
        const float gt = rnd_bf16(1.f / (1.f + __expf(-gs)));
        if (lane == 0) st_sc1(rws, WS_G + (h * 8 + b) * 16, tag_bf16(f32x4{gt, 1.f - gt, 0.f, 0.f}, T));
      }
      __builtin_amdgcn_s_setprio(0);
      sleep_until(now() + 800);
      {
        v4u u[4];
        const int base = WS_QB + (b * 32 + (h & 24)) * 512 + lane * 16;
        poll<0xffu, 4, 16>(rws, [&](int k) { return base + 1024 * k; }, u, T);
#pragma unroll
        for (int k = 0; k < 4; ++k) *(v2u*)(ldu + L_QB + 128 * k + 2 * lane) = pack_hi(u[k]);
        lds_post(flag + 1);
      }
      STAMP(3);
      sleep_until(tstart + 4400);
      lds_wait(flag + 4); lds_wait(flag + 5); lds_wait(flag + 6); lds_wait(flag + 7);
      STAMP(4);
#pragma unroll
      for (int k = 0; k < 4; ++k) {
        const int hd = 2 * k + (lane >> 5), n4 = 4 * (lane & 31);
        float M, L; f32x4 sum;
        merge_recs<4>(lds + L_MLB + hd * 2, 16, lds + L_RB + hd * RSTR + n4, 8 * RSTR, M, L, sum);
        const int rec = (b * 32 + (h & 24) + hd) * 8 + (h & 7);
        st_sc1(rws, WS_BACC + (rec * 128 + n4) * 4, tag_f32<0xffu>(sum, T));
        if (n4 == 0) {
          const float Mh = u2f(f2u(M) & 0xffffff00u);
          const v4u r = {f2u(Mh), f2u(M - Mh), f2u(L) + 0x80u, 0u};
          st_sc1(rws, WS_BML + rec * 16, tag_raw<0xffu>(r, T));
        }
      }
      sleep_until(now() + 800);
      {
        v4u u[5];
        const int rec = me * 8;
        const int hf = lane >> 5;
        poll<0xffu, 5, 64>(rws, [&](int k) { return k < 4 ? WS_BACC + ((rec + 2 * k + hf) * 128 + 4 * (lane & 31)) * 4 : WS_BML + (rec + c7) * 16; }, u, T);
        const f32x4 r = untag<0xffu>(u[4]);
        const float ms = r.x + r.y;
        float M = fmaxf(ms, dpp<0x124>(ms)); M = fmaxf(M, dpp<0x122>(M)); M = fmaxf(M, dpp<0x121>(M));
        const float wt = __builtin_amdgcn_exp2f((ms - M) * 1.4426950408889634f);
        float L = wt * r.z; L += dpp<0x124>(L); L += dpp<0x122>(L); L += dpp<0x121>(L);
        f32x4 sum = {0.f, 0.f, 0.f, 0.f};
#pragma unroll
        for (int k = 0; k < 4; ++k) sum += u2f((unsigned)__builtin_amdgcn_ds_bpermute((2 * k + hf) * 4, (int)f2u(wt))) * untag<0xffu>(u[k]);
#pragma unroll
        for (int i = 0; i < 4; ++i) sum[i] = half_sum(sum[i]);
        if (lane < 32) st_sc1(rws, WS_O + (((32 + h) * 8 + b) * 128 + rec_n0) * 4, tag_bf16(sum * (1.f / L), T));
      }
      STAMP(5);
      sleep_until(now() + 800);
      {
        v4u u[5];
        const int base = WS_O + (32 + h) * 4096 + lane * 16;
        poll<0xffu, 5, 64>(rws, [&](int k) { return k < 4 ? base + 1024 * k : WS_G + (h * 8 + c7) * 16; }, u, T);
#pragma unroll
        for (int k = 0; k < 4; ++k) *(v2u*)(ldu + L_OB + 128 * k + 2 * lane) = pack_hi(u[k]);
        if (lane < 8) *(v2u*)(ldu + L_G + 2 * lane) = v2u{u[4].x & 0xffff0000u, u[4].y & 0xffff0000u};
        lds_post(flag + 22);
      }
      STAMP(6);
    }
  } else {
    {
      const void* tp = w == 0 ? (const char*)p.vA + ((size_t)me << 20) : w == 1 ? (const char*)p.kB + ((size_t)me << 17)
                     : w == 2 ? (const char*)p.vB + ((size_t)me << 17) : (const char*)p.out;
      const void* tq = (const char*)(w & 1 ? p.woA : p.woB) + (size_t)(128 * h) * 2048 + 256 * b;
      const unsigned t0 = __builtin_amdgcn_raw_buffer_load_b32(mk_rsrc(tp, 0, 64), 0, 0, AUX_NT);
      const unsigned t1 = __builtin_amdgcn_raw_buffer_load_b32(mk_rsrc(tq, 0, w < 2 ? 64 : 0), 0, 0, AUX_NT);
      asm volatile("" :: "v"(t0), "v"(t1), "v"(cwarm));
    }
    __builtin_amdgcn_s_setprio(2);
    Pacer pc;
    pc.t0 = (unsigned)__builtin_amdgcn_readfirstlane(ldu[L_T0]);
    pc.acc = PT0; pc.inc = P1; pc.off = 0; pc.goal = 0; pc.mine = flag + 32 + w; pc.cmd = flag + 36;
    const rsrc_t rkA = mk_rsrc(p.kA, (size_t)(me & ~1) << 20, 2 << 20);
    const rsrc_t rvA = mk_rsrc(p.vA, (size_t)(me & ~1) << 20, 2 << 20);
    const int lin = lane * 16;
    auto ldR = [&](rsrc_t r, int soff) { return [=](int j) { return ld_nt(r, lin + 1024 * j, soff); }; };
    unsigned* const sc = ldu + L_SC + g * 4;
    v4u X[8], Y[8];
    unsigned T;
    v4u q1;
    int myK = 0;
    auto kblk = [&](int n) { return k_block(n, w, role, myK); };
    auto emit = [&](int n, v4u sv) { if (c == 0) *(v4u*)(sc + kblk(n) * 16) = sv; };
    {
      v4u X1[8], Y1[8], X2[8], Y2[8];
      auto pre = [&](v4u (&R)[8], int n) {
#pragma unroll
        for (int j = 0; j < 8; ++j) { if ((j & 3) == 0) pc.tick<false>(); wait_vm<WIN - 1>(); R[j] = ldR(rkA, kblk(n) * 8192)(j); SB(); }
      };
      pre(X, 0); pre(Y, 1); pre(X1, 2); pre(Y1, 3); pre(X2, 4); pre(Y2, 5);
      PIN();
      STAMP(1);
      lds_wait(flag + 0);
      q1 = *(const v4u*)(ldu + L_QA + 4 * c);
      T = (unsigned)__builtin_amdgcn_readfirstlane(ldu[L_T]);
      STAMP(2);
      emit(0, k_part<true>(X, q1, pc, ldR(rkA, kblk(6) * 8192)));
      emit(2, k_part<false>(X1, q1, pc, ldR(rkA, 0)));
      emit(3, k_part<false>(Y1, q1, pc, ldR(rkA, 0)));
      emit(1, k_part<true>(Y, q1, pc, ldR(rkA, kblk(7) * 8192)));
      emit(4, k_part<false>(X2, q1, pc, ldR(rkA, 0)));
      emit(5, k_part<false>(Y2, q1, pc, ldR(rkA, 0)));
    }
    STAMP(3);
    myK = lds_wait(flag + 2) - 1;
    pc.inc = P2;
    STAMP(4);
    for (int n = 6; n < 30; n += 2) {
      if (n == 20) { lds_wait(flag + 12); q1 = *(const v4u*)(ldu + L_QA + 64 + 4 * c); }
      emit(n, k_part<true>(X, q1, pc, ldR(rkA, kblk(n + 2) * 8192)));
      emit(n + 1, k_part<true>(Y, q1, pc, ldR(rkA, kblk(n + 3) * 8192)));
    }
    f32x4 acc[8];
    int vb;
    auto vblk = [&](int v) { return (vb + 16 * (v >> 1) + 2 * (v & 1)) & 255; };
    {
      const rsrc_t rkB = mk_rsrc(p.kB, (size_t)me << 17, 1 << 17);
      const rsrc_t rvB = mk_rsrc(p.vB, (size_t)me << 17, 1 << 17);
      auto wb = [&](int n) { return blind_block(n, w) * 8192; };
      pc.inc = PBB;
      STAMP(5);
      emit(30, k_part<true>(X, q1, pc, ldR(rkB, wb(0))));
      emit(31, k_part<true>(Y, q1, pc, ldR(rkB, wb(1))));
      lds_post(flag + 16 + w);
      lds_wait(flag + 1);
      v4u QB[4], PB[4];
#pragma unroll
      for (int k = 0; k < 4; ++k) QB[k] = *(const v4u*)(ldu + L_QB + 64 * c7 + 4 * (4 * k + g));
      unsigned* const tile = ldu + L_KT + w * 2048;
      PB[0] = k_part8(X, QB, tile, g, c, pc, ldR(rkB, wb(2)));
      PB[1] = k_part8(Y, QB, tile, g, c, pc, ldR(rkB, wb(3)));
      PB[2] = k_part8(X, QB, tile, g, c, pc, ldR(rvB, wb(0)));
      PB[3] = k_part8(Y, QB, tile, g, c, pc, ldR(rvB, wb(1)));
      float mB, lB = 0.f;
      {
        float e[32];
#pragma unroll
        for (int i = 0; i < 16; ++i) { e[2 * i] = u2f(PB[i >> 2][i & 3] << 16); e[2 * i + 1] = u2f(PB[i >> 2][i & 3] & 0xffff0000u); }
        mB = e[0];
#pragma unroll
        for (int i = 1; i < 32; ++i) mB = fmaxf(mB, e[i]);
        mB = xrow_max(mB);
#pragma unroll
        for (int i = 0; i < 32; ++i) { e[i] = __builtin_amdgcn_exp2f((e[i] - mB) * 1.4426950408889634f); lB += e[i]; }
#pragma unroll
        for (int i = 0; i < 16; ++i) PB[i >> 2][i & 3] = pk_bf16(e[2 * i], e[2 * i + 1]);
        lB = xrow_sum(lB);
        if (lane < 8) *(f32x2*)(lds + L_MLB + (w * 8 + lane) * 2) = f32x2{mB, lB};
      }
      acc_zero(acc);
      STAMP(6);
      v_part<true>(X, PB[0], acc, pc, ldR(rvB, wb(2)));
      v_part<true>(Y, PB[1], acc, pc, ldR(rvB, wb(3)));
      vb = (role ^ 1) * 128 + 4 * w + (lds_wait(flag + 3) - 1);
      pc.inc = P2;
      v_part<true>(X, PB[2], acc, pc, ldR(rvA, vblk(0) * 8192));
      v_part<true>(Y, PB[3], acc, pc, ldR(rvA, vblk(1) * 8192));
#pragma unroll
      for (int k = 0; k < 4; ++k) *(f32x4*)(lds + L_RB + (w * 8 + c7) * RSTR + k * 32 + g * 8 + hi * 4) = tile_vec(acc, k, hi);
      lds_post(flag + 4 + w);
    }
    auto prob = [&](int v) { return *(const v4u*)(sc + vblk(v) * 16); };
    auto dump_one = [&](int rows) {
      if (c7 == 0) {
#pragma unroll
        for (int k = 0; k < 4; ++k) *(f32x4*)(lds + rows + w * 128 + k * 32 + g * 8 + hi * 4) = tile_vec(acc, k, hi);
      }
    };
    acc_zero(acc);
    STAMP(7);
    lds_wait(flag + 24);
    STAMP(8);
    for (int v = 0; v < 30; v += 2) {
      if (v == 16) {
        STAMP(9);
        dump_one(L_RF);
        lds_post(flag + 8 + w);
        acc_zero(acc);
        lds_wait(flag + 25);
        pc.inc = P3;
      }
      v_part<true>(X, prob(v), acc, pc, ldR(rvA, vblk(v + 2) * 8192));
      if (v == 28) flag[26] = 1u;
      v_part<true>(Y, prob(v + 1), acc, pc, ldR(rvA, vblk(v + 3) * 8192));
    }
    STAMP(10);
    const rsrc_t roa = mk_rsrc(p.woA, (size_t)(128 * h) * 2048 + 256 * b, 127 * 2048 + 256);
    const rsrc_t rob = mk_rsrc(p.woB, (size_t)(128 * h) * 2048 + 256 * b, 127 * 2048 + 256);
    const int wo = (32 * w + 8 * g) * 2048 + 16 * c;
    v4u K[8], V[8];
    auto ldw = [&](v4u (&R)[8], rsrc_t r, int j0) {
      pc.tick<false>();
#pragma unroll
      for (int j = j0; j < j0 + 4; ++j) { R[j] = ld_nt(r, wo, 2048 * j); SB(); }
      PIN();
    };
    pc.inc = PW;
    {
      const v4u p30 = prob(30), p31 = prob(31);
      v_part<false>(X, p30, acc, pc, ldR(rvA, 0));
      PIN();
      ldw(K, rob, 0);
      v_part<false>(Y, p31, acc, pc, ldR(rvA, 0));
    }
    STAMP(11);
    dump_one(L_RS);
    lds_post(flag + 13 + (w == 3 ? 10 : w));
    PIN();
    ldw(K, rob, 4); ldw(V, roa, 0); ldw(V, roa, 4);
    STAMP(12);
    {
      f32x4 y[8];
      lds_wait(flag + 22);
      const f32x2 gt = *(const f32x2*)(lds + L_G + 2 * c7);
      f32x4 yv[4];
      {
        const v4u oB = *(const v4u*)(ldu + L_OB + 64 * c7 + 16 * w + 4 * g);
        v4u a[8];
#pragma unroll
        for (int q = 0; q < 4; ++q) tr_pair(K[2 * q], K[2 * q + 1], a, q);
#pragma unroll
        for (int e = 0; e < 8; ++e) y[e] = mfma(a[e], oB, f32x4{0.f, 0.f, 0.f, 0.f});
#pragma unroll
        for (int k = 0; k < 4; ++k) yv[k] = gt.y * tile_vec(y, k, hi);
      }
      v4u a[8];
#pragma unroll
      for (int q = 0; q < 4; ++q) tr_pair(V[2 * q], V[2 * q + 1], a, q);
      asm volatile("" : "+v"(a[0]), "+v"(a[1]), "+v"(a[2]), "+v"(a[3]), "+v"(a[4]), "+v"(a[5]), "+v"(a[6]), "+v"(a[7]), "+v"(yv[0]), "+v"(yv[1]), "+v"(yv[2]), "+v"(yv[3]) :: "memory");
      {
        for (unsigned spin = 0; __builtin_amdgcn_readfirstlane(flag[27]) == 0u && spin < (SPIN_CAP << 4); ++spin) {}
        asm volatile("" ::: "memory");
        const v4u oA = *(const v4u*)(ldu + L_OA + 64 * c7 + 16 * w + 4 * g);
#pragma unroll
        for (int e = 0; e < 8; ++e) y[e] = mfma(a[e], oA, f32x4{0.f, 0.f, 0.f, 0.f});
      }
      STAMP(13);
#pragma unroll
      for (int k = 0; k < 4; ++k) *(f32x4*)(lds + L_RY + ((w * 4 + k) * 64 + lane) * 4) = yv[k] + gt.x * tile_vec(y, k, hi);
      wg_barrier();
      f32x4 sum = *(const f32x4*)(lds + L_RY + (w * 64 + lane) * 4);
#pragma unroll
      for (int ww = 1; ww < 4; ++ww) sum += *(const f32x4*)(lds + L_RY + ((ww * 4 + w) * 64 + lane) * 4);
      st_sc1(rws, WS_Y + (((b * 32 + c7 * 4 + w) * 32 + h) * 32 + g * 8 + hi * 4) * 4, tag_f32<0xffu>(sum, T));
    }
    STAMP(14);
    if (w == 0) {
      if (YDELAY < 0) drain(); else sleep_until(now() + YDELAY);
      v4u u[4];
      const int yb = WS_Y + me * 4096 + lane * 16;
      poll<0xffu, 4>(rws, [&](int k) { return yb + 1024 * k; }, u, T);
      f32x4 sum = (untag<0xffu>(u[0]) + untag<0xffu>(u[1])) + (untag<0xffu>(u[2]) + untag<0xffu>(u[3]));
#pragma unroll
      for (int i = 0; i < 4; ++i) sum[i] = xrow_sum(sum[i] + dpp<0x128>(sum[i]));
      const rsrc_t ro = mk_rsrc(p.out, 0, 8 * 1024 * 2);
      if (lane < 8) {
        const v2u o = {pk_bf16(sum[0], sum[1]), pk_bf16(sum[2], sum[3])};
        __builtin_amdgcn_raw_buffer_store_b64(o, ro, ((h >> 2) * 1024 + 128 * b + tile_n0(h & 3, lane >> 1, lane & 1)) * 2, 0, 0);
      }
      if (blockIdx.x == 0 && lane == 0) __builtin_amdgcn_raw_buffer_store_b32(T, rws, WS_EPOCH, 0, AUX_SC1);
    }
    STAMP(15);
    asm volatile(".Lfd_code_end:" ::: "memory");
  }
}

extern "C" void kernel_launch(void* const* d_in, const int* in_sizes, int n_in, void* d_out, int out_size,
                              void* d_ws, size_t ws_size, hipStream_t stream) {
  Params p;
  p.x = d_in[0]; p.kA = d_in[1]; p.vA = d_in[2]; p.wqA = d_in[3]; p.woA = d_in[4];
  p.kB = d_in[6]; p.vB = d_in[7]; p.wqB = d_in[8]; p.woB = d_in[9]; p.gate = d_in[10];
  p.out = d_out; p.ws = d_ws;
  hipLaunchKernelGGL(fused_decode, dim3(256), dim3(512), 0, stream, p);
}
```
